```python
import torch

def _softmax(x, axis=-1):
    return torch.softmax(x, dim=axis)
def _gelu(x):
    return torch.nn.functional.gelu(x, approximate="tanh")
def _silu(x):
    return torch.nn.functional.silu(x)
def _sigmoid(x):
    return torch.sigmoid(x)
def _layernorm(x, axis=-1, eps=1e-5):
    m = x.mean(dim=axis, keepdim=True)
    v = x.var(dim=axis, keepdim=True, unbiased=False)
    return (x - m) / torch.sqrt(v + eps)
def _scatter_add(tgt, idx, src, axis=0):
    return tgt.clone().index_add_(axis, idx, src)
def _segment_sum(src, seg, n):
    out = torch.zeros((n,) + src.shape[1:], dtype=src.dtype)
    return out.index_add_(0, seg, src)
def _pad(x, pw):
    flat = tuple(v for pair in reversed(pw) for v in pair)
    return torch.nn.functional.pad(x, flat)

kernel_name = 'standalone__w4a16_gemm_dequant__prompt'

def setup_inputs():

    _w = (lambda a: (lambda t: t.to(torch.bfloat16) if t.is_floating_point() else t)(torch.as_tensor(a)))
    B, S, D, F = 1, 2048, 576, 4608
    G = -1 if -1 > 0 else D
    D = (D // G) * G
    ng = D // G
    qw = torch.randint(0, 16, (D, F)).to(torch.int32)
    return {
        "x": _w((lambda *s: torch.randn(*s, dtype=torch.bfloat16) * 0.05)(B, S, D)),
        "qw": _w(qw),
        "scales": _w(torch.abs((lambda *s: torch.randn(*s, dtype=torch.bfloat16) * 0.05)(ng, F)) + 0.01),
        "zeros": _w(torch.full((ng, F), 8.0, dtype=float)),
    }

def reference(x, qw, scales, zeros):
    B, S, D = x.shape
    ng, F = scales.shape
    G = D // ng
    s = (lambda x, n, axis: torch.repeat_interleave(x, n, dim=axis))(scales, G, axis=0)
    z = (lambda x, n, axis: torch.repeat_interleave(x, n, dim=axis))(zeros, G, axis=0)
    w = ((lambda x, dt: x.to(dt))(qw, x.dtype) - z) * s
    return x @ w
```

```cpp
#include <hip/hip_runtime.h>
#include <hip/hip_bf16.h>
#include <stdint.h>

typedef __attribute__((ext_vector_type(8))) short bf16x8;
typedef __attribute__((ext_vector_type(4))) float f32x4;
typedef __attribute__((ext_vector_type(4))) int i32x4;
typedef __attribute__((ext_vector_type(2))) unsigned int u32x2;

constexpr int M_ = 2048, K_ = 576, N_ = 4608;

__device__ __forceinline__ unsigned cvt_pk(float lo, float hi) {
    typedef __attribute__((ext_vector_type(2))) float f32x2;
    typedef __attribute__((ext_vector_type(2))) __bf16 bf16x2;
    f32x2 v = {lo, hi};
    bf16x2 r = __builtin_convertvector(v, bf16x2);
    return __builtin_bit_cast(unsigned, r);
}
__device__ __forceinline__ float bf2f(unsigned short h) { return __builtin_bit_cast(float, (unsigned)h << 16); }

__global__ __launch_bounds__(256) void k_simple(const unsigned short* __restrict__ x, const int* __restrict__ q,
                                                const unsigned short* __restrict__ sc, const unsigned short* __restrict__ zr,
                                                unsigned short* __restrict__ out) {
    int wid = threadIdx.x >> 6, lane = threadIdx.x & 63;
    int gw = blockIdx.x * 4 + wid;
    int mt = gw % 32, nt = gw / 32;
    int m0 = mt * 64, n0 = nt * 64;
    int c = lane & 15, g = lane >> 4;
    float s[4], zs[4];
    for (int i = 0; i < 4; ++i) {
        float sv = bf2f(sc[n0 + 4 * c + i]), zv = bf2f(zr[n0 + 4 * c + i]);
        s[i] = sv; zs[i] = -zv * sv;
    }
    f32x4 acc[4][4] = {};
    for (int k0 = 0; k0 < K_; k0 += 32) {
        i32x4 qv[8];
        for (int j = 0; j < 8; ++j)
            qv[j] = *(const i32x4*)(q + (size_t)(k0 + 8 * g + j) * N_ + n0 + 4 * c);
        bf16x8 xf[4];
        for (int t = 0; t < 4; ++t)
            xf[t] = *(const bf16x8*)(x + (size_t)(m0 + 16 * t + c) * K_ + k0 + 8 * g);
        for (int i = 0; i < 4; ++i) {
            unsigned w[4];
            for (int j = 0; j < 4; ++j) {
                float a = fmaf((float)qv[2 * j][i], s[i], zs[i]);
                float b = fmaf((float)qv[2 * j + 1][i], s[i], zs[i]);
                w[j] = cvt_pk(a, b);
            }
            bf16x8 wf;
            __builtin_memcpy(&wf, w, 16);
            for (int t = 0; t < 4; ++t)
                acc[i][t] = __builtin_amdgcn_mfma_f32_16x16x32_bf16(wf, xf[t], acc[i][t], 0, 0, 0);
        }
    }
    for (int t = 0; t < 4; ++t) {
        int m = m0 + 16 * t + c;
        for (int r = 0; r < 4; ++r) {
            u32x2 o;
            o[0] = cvt_pk(acc[0][t][r], acc[1][t][r]);
            o[1] = cvt_pk(acc[2][t][r], acc[3][t][r]);
            *(u32x2*)(out + (size_t)m * N_ + n0 + 4 * (4 * g + r)) = o;
        }
    }
}

extern "C" void kernel_launch(void* const* d_in, const int* in_sizes, int n_in,
                              void* d_out, int out_size, void* d_ws, size_t ws_size,
                              hipStream_t stream) {
    const unsigned short* x = (const unsigned short*)d_in[0];
    const int* q = (const int*)d_in[1];
    const unsigned short* sc = (const unsigned short*)d_in[2];
    const unsigned short* zr = (const unsigned short*)d_in[3];
    k_simple<<<576, 256, 0, stream>>>(x, q, sc, zr, (unsigned short*)d_out);
}
```

```cpp
#include <hip/hip_runtime.h>
#include <hip/hip_bf16.h>
#include <stdint.h>

typedef __attribute__((ext_vector_type(8))) short bf16x8;
typedef __attribute__((ext_vector_type(4))) float f32x4;
typedef __attribute__((ext_vector_type(4))) int i32x4;
typedef __attribute__((ext_vector_type(2))) unsigned int u32x2;

constexpr int M_ = 2048, K_ = 576, N_ = 4608;

__device__ __forceinline__ unsigned cvt_pk(float lo, float hi) {
    typedef __attribute__((ext_vector_type(2))) float f32x2;
    typedef __attribute__((ext_vector_type(2))) __bf16 bf16x2;
    f32x2 v = {lo, hi};
    bf16x2 r = __builtin_convertvector(v, bf16x2);
    return __builtin_bit_cast(unsigned, r);
}
__device__ __forceinline__ float bf2f(unsigned short h) { return __builtin_bit_cast(float, (unsigned)h << 16); }

__global__ __launch_bounds__(512) void k_simple(const unsigned short* __restrict__ x, const int* __restrict__ q,
                                                const unsigned short* __restrict__ sc, const unsigned short* __restrict__ zr,
                                                unsigned short* __restrict__ out) {
    const unsigned long long t0_ = __builtin_amdgcn_s_memrealtime();
    int wid = threadIdx.x >> 6, lane = threadIdx.x & 63;
    for (int gw = blockIdx.x * 8 + wid; gw < 2304; gw += 2048) {
    int mt = gw % 32, nt = gw / 32;
    int m0 = mt * 64, n0 = nt * 64;
    int c = lane & 15, g = lane >> 4;
    float s[4], zs[4];
    for (int i = 0; i < 4; ++i) {
        float sv = bf2f(sc[n0 + 4 * c + i]), zv = bf2f(zr[n0 + 4 * c + i]);
        s[i] = sv; zs[i] = -zv * sv;
    }
    f32x4 acc[4][4] = {};
    for (int k0 = 0; k0 < K_; k0 += 32) {
        i32x4 qv[8];
        for (int j = 0; j < 8; ++j)
            qv[j] = *(const i32x4*)(q + (size_t)(k0 + 8 * g + j) * N_ + n0 + 4 * c);
        bf16x8 xf[4];
        for (int t = 0; t < 4; ++t)
            xf[t] = *(const bf16x8*)(x + (size_t)(m0 + 16 * t + c) * K_ + k0 + 8 * g);
        for (int i = 0; i < 4; ++i) {
            unsigned w[4];
            for (int j = 0; j < 4; ++j) {
                float a = fmaf((float)qv[2 * j][i], s[i], zs[i]);
                float b = fmaf((float)qv[2 * j + 1][i], s[i], zs[i]);
                w[j] = cvt_pk(a, b);
            }
            bf16x8 wf;
            __builtin_memcpy(&wf, w, 16);
            for (int t = 0; t < 4; ++t)
                acc[i][t] = __builtin_amdgcn_mfma_f32_16x16x32_bf16(wf, xf[t], acc[i][t], 0, 0, 0);
        }
    }
    for (int t = 0; t < 4; ++t) {
        int m = m0 + 16 * t + c;
        for (int r = 0; r < 4; ++r) {
            u32x2 o;
            o[0] = cvt_pk(acc[0][t][r], acc[1][t][r]);
            o[1] = cvt_pk(acc[2][t][r], acc[3][t][r]);
            __builtin_nontemporal_store(o, (u32x2*)(out + (size_t)m * N_ + n0 + 4 * (4 * g + r)));
        }
    }
    }
    while (__builtin_amdgcn_s_memrealtime() - t0_ < 20000) __builtin_amdgcn_s_sleep(2);
}

extern "C" void kernel_launch(void* const* d_in, const int* in_sizes, int n_in,
                              void* d_out, int out_size, void* d_ws, size_t ws_size,
                              hipStream_t stream) {
    const unsigned short* x = (const unsigned short*)d_in[0];
    const int* q = (const int*)d_in[1];
    const unsigned short* sc = (const unsigned short*)d_in[2];
    const unsigned short* zr = (const unsigned short*)d_in[3];
    k_simple<<<256, 512, 0, stream>>>(x, q, sc, zr, (unsigned short*)d_out);
}
```

```cpp
#include <hip/hip_runtime.h>
#include <hip/hip_bf16.h>
#include <stdint.h>

#define LDS_BYTES 113152

__global__ __launch_bounds__(768, 3) void w4a16_gemm_kernel(const unsigned short* x, const int* q, const unsigned short* sc,
                                                            const unsigned short* zr, unsigned short* out) {
    __shared__ __attribute__((aligned(16))) char smem[LDS_BYTES];
    unsigned tid = threadIdx.x;
    asm volatile(
        "s_load_dwordx8 s[8:15], %[karg], 0x0\n\t"
        "s_load_dwordx2 s[16:17], %[karg], 0x20\n\t"
        "v_readfirstlane_b32 s48, v167\n\t"
        "v_and_b32 v166, 63, v167\n\t"
        "v_lshlrev_b32 v166, 4, v166\n\t"
        "s_lshr_b32 s49, %[bid], 6\n\t"
        "s_bfe_u32 s50, %[bid], 0x30003\n\t"
        "s_and_b32 s51, %[bid], 7\n\t"
        "s_lshr_b32 s48, s48, 6\n\t"
        "s_lshr_b32 s59, s49, 1\n\t"
        "s_lshl_b32 s52, s49, 1\n\t"
        "s_add_u32 s52, s52, s59\n\t"
        "s_lshl_b32 s52, s52, 6\n\t"
        "s_lshl_b32 s53, s59, 5\n\t"
        "s_add_u32 s53, s53, 0x100\n\t"
        "s_add_u32 s86, s50, s49\n\t"
        "s_and_b32 s86, s86, 3\n\t"
        "s_mov_b32 s84, 0x86534210\n\t"
        "s_mov_b32 s85, 7\n\t"
        "s_cmp_eq_u32 s86, 1\n\t"
        "s_cselect_b32 s84, 0x76453201, s84\n\t"
        "s_cselect_b32 s85, 8, s85\n\t"
        "s_cmp_eq_u32 s86, 2\n\t"
        "s_cselect_b32 s84, 0x75643120, s84\n\t"
        "s_cselect_b32 s85, 8, s85\n\t"
        "s_cmp_eq_u32 s86, 3\n\t"
        "s_cselect_b32 s84, 0x67542310, s84\n\t"
        "s_cselect_b32 s85, 8, s85\n\t"
        "s_cmp_gt_u32 s48, 3\n\t"
        "s_cbranch_scc1 8f\n\t"
        "s_setprio 3\n\t"
        "s_lshl_b32 s44, s52, 2\n\t"
        "s_lshl_b32 s45, s53, 2\n\t"
        "v_lshrrev_b32 v165, 8, v166\n\t"
        "v_mul_u32_u24 v165, 147456, v165\n\t"
        "v_and_b32 v148, 0xf0, v166\n\t"
        "v_add3_u32 v144, v165, v148, s44\n\t"
        "v_lshrrev_b32 v165, 7, v166\n\t"
        "v_mul_u32_u24 v165, 147456, v165\n\t"
        "v_and_b32 v149, 0x70, v166\n\t"
        "v_add3_u32 v145, v165, v149, s45\n\t"
        "v_add_u32 v149, 0x200, v149\n\t"
        "s_lshl_b32 s88, s48, 3\n\t"
        "s_mul_i32 s59, s51, 2304\n\t"
        "s_sub_u32 s22, 10616832, s59\n\t"
        "s_mov_b32 s23, 0x00020000\n\t"
        "s_lshr_b32 s58, s59, 1\n\t"
        "s_movk_i32 s30, 1152\n\t"
        "s_mov_b32 s31, 0x00020000\n\t"
        "s_movk_i32 s34, 1152\n\t"
        "s_mov_b32 s35, 0x00020000\n\t"
        "s_lshr_b32 s86, 0x1204000, s88\n\t"
        "s_bfe_u32 s100, s86, 0x20005\n\t"
        "s_and_b32 s46, s86, 31\n\t"
        "s_lshr_b32 s86, s46, 1\n\t"
        "s_lshl_b32 s86, s86, 2\n\t"
        "s_lshr_b64 s[86:87], s[84:85], s86\n\t"
        "s_and_b32 s86, s86, 15\n\t"
        "s_lshl_b32 s86, s86, 1\n\t"
        "s_and_b32 s87, s46, 1\n\t"
        "s_or_b32 s86, s86, s87\n\t"
        "s_cmp_lt_u32 s46, 18\n\t"
        "s_cselect_b32 s86, s86, 64\n\t"
        "s_mul_i32 s92, s86, 589824\n\t"
        "s_lshr_b32 s86, 0x3020100, s88\n\t"
        "s_and_b32 s86, s86, 31\n\t"
        "s_lshl_b32 s96, s86, 12\n\t"
        "s_and_b32 s86, s100, 1\n\t"
        "s_lshl_b32 s86, s86, 8\n\t"
        "s_add_u32 s92, s92, s86\n\t"
        "s_cmp_eq_u32 s100, 2\n\t"
        "s_cselect_b64 vcc, -1, 0\n\t"
        "v_cndmask_b32 v146, v144, v145, vcc\n\t"
        "v_cndmask_b32 v150, v148, v149, vcc\n\t"
        "v_add_u32 v150, s86, v150\n\t"
        "s_lshr_b32 s47, s86, 1\n\t"
        "v_bfe_u32 v156, v146, 1, 13\n\t"
        "s_lshl_b32 s44, s48, 6\n\t"
        "s_add_u32 s44, s44, s52\n\t"
        "s_cmp_lt_u32 s48, 2\n\t"
        "s_cselect_b32 s44, s44, s53\n\t"
        "s_lshl_b32 s44, s44, 1\n\t"
        "v_lshrrev_b32 v160, 3, v166\n\t"
        "s_waitcnt lgkmcnt(0)\n\t"
        "s_add_u32 s20, s10, s59\n\t"
        "s_addc_u32 s21, s11, 0\n\t"
        "s_add_u32 s28, s12, s58\n\t"
        "s_addc_u32 s29, s13, 0\n\t"
        "s_add_u32 s32, s14, s58\n\t"
        "s_addc_u32 s33, s15, 0\n\t"
        "buffer_load_dwordx4 v[0:3], v146, s[20:23], s92 offen\n\t"
        "s_add_u32 s58, s92, 18432\n\t"
        "buffer_load_dwordx4 v[4:7], v146, s[20:23], s58 offen\n\t"
        "s_add_u32 s58, s92, 36864\n\t"
        "buffer_load_dwordx4 v[8:11], v146, s[20:23], s58 offen\n\t"
        "s_add_u32 s58, s92, 55296\n\t"
        "buffer_load_dwordx4 v[12:15], v146, s[20:23], s58 offen\n\t"
        "s_add_u32 s58, s92, 73728\n\t"
        "buffer_load_dwordx4 v[16:19], v146, s[20:23], s58 offen\n\t"
        "s_add_u32 s58, s92, 92160\n\t"
        "buffer_load_dwordx4 v[20:23], v146, s[20:23], s58 offen\n\t"
        "s_add_u32 s58, s92, 110592\n\t"
        "buffer_load_dwordx4 v[24:27], v146, s[20:23], s58 offen\n\t"
        "s_add_u32 s58, s92, 129024\n\t"
        "buffer_load_dwordx4 v[28:31], v146, s[20:23], s58 offen\n\t"
        "buffer_load_dwordx2 v[154:155], v156, s[28:31], s47 offen\n\t"
        "buffer_load_dwordx2 v[156:157], v156, s[32:35], s47 offen\n\t"
        "buffer_load_ushort v158, v160, s[28:31], s44 offen\n\t"
        "buffer_load_ushort v159, v160, s[32:35], s44 offen\n\t"
        "s_waitcnt vmcnt(1)\n\t"
        "v_lshlrev_b32 v158, 16, v158\n\t"
        "s_waitcnt vmcnt(0)\n\t"
        "v_lshlrev_b32 v159, 16, v159\n\t"
        "v_mul_f32_e64 v159, -v159, v158\n\t"
        "v_mul_f32 v158, 0x4b800000, v158\n\t"
        "s_lshl_b32 s59, s48, 8\n\t"
        "s_cmp_lt_u32 s48, 3\n\t"
        "s_cselect_b32 s59, s59, -4096\n\t"
        "s_add_u32 s59, s59, 45056\n\t"
        "v_lshrrev_b32 v160, 2, v166\n\t"
        "v_add_u32 v160, s59, v160\n\t"
        "ds_write_b32 v160, v158 offset:0\n\t"
        "ds_write_b32 v160, v159 offset:768\n\t"
        "s_waitcnt lgkmcnt(0)\n\t"
        "s_cmp_lt_u32 s48, 2\n\t"
        "s_cbranch_scc1 3f\n\t"
        "s_barrier\n\t"
        "3:\n\t"
        "v_lshlrev_b32 v128, 16, v154\n\t"
        "v_and_b32 v129, 0xffff0000, v154\n\t"
        "v_lshlrev_b32 v130, 16, v155\n\t"
        "v_and_b32 v131, 0xffff0000, v155\n\t"
        "v_lshlrev_b32 v132, 16, v156\n\t"
        "v_and_b32 v133, 0xffff0000, v156\n\t"
        "v_lshlrev_b32 v134, 16, v157\n\t"
        "s_lshr_b32 s86, 0x22420221, s88\n\t"
        "s_bfe_u32 s101, s86, 0x20005\n\t"
        "s_and_b32 s13, s86, 31\n\t"
        "s_lshr_b32 s86, s13, 1\n\t"
        "s_lshl_b32 s86, s86, 2\n\t"
        "s_lshr_b64 s[86:87], s[84:85], s86\n\t"
        "s_and_b32 s86, s86, 15\n\t"
        "s_lshl_b32 s86, s86, 1\n\t"
        "s_and_b32 s87, s13, 1\n\t"
        "s_or_b32 s86, s86, s87\n\t"
        "s_cmp_lt_u32 s13, 18\n\t"
        "s_cselect_b32 s86, s86, 64\n\t"
        "s_mul_i32 s93, s86, 589824\n\t"
        "s_lshr_b32 s86, 0x7060504, s88\n\t"
        "s_and_b32 s86, s86, 31\n\t"
        "s_lshl_b32 s97, s86, 12\n\t"
        "s_and_b32 s86, s101, 1\n\t"
        "s_lshl_b32 s86, s86, 8\n\t"
        "s_add_u32 s93, s93, s86\n\t"
        "v_and_b32 v135, 0xffff0000, v157\n\t"
        "v_mul_f32_e64 v132, -v132, v128\n\t"
        "v_mul_f32_e64 v133, -v133, v129\n\t"
        "v_mul_f32_e64 v134, -v134, v130\n\t"
        "v_mul_f32_e64 v135, -v135, v131\n\t"
        "v_mul_f32 v128, 0x4b800000, v128\n\t"
        "v_mul_f32 v129, 0x4b800000, v129\n\t"
        "s_cmp_eq_u32 s101, 2\n\t"
        "s_cselect_b64 vcc, -1, 0\n\t"
        "v_cndmask_b32 v146, v144, v145, vcc\n\t"
        "v_cndmask_b32 v151, v148, v149, vcc\n\t"
        "v_add_u32 v151, s86, v151\n\t"
        "v_mul_f32 v130, 0x4b800000, v130\n\t"
        "v_mul_f32 v131, 0x4b800000, v131\n\t"
        "s_cmp_eq_u32 s100, 2\n\t"
        "s_cselect_b32 s89, 32, 0\n\t"
        "s_cselect_b32 s90, 64, 0\n\t"
        "s_cselect_b32 s91, 0x60, 0\n\t"
        "v_add_u32 v147, s96, v166\n\t"
        "v_fma_mix_f32 v0, v0, v128, v132 op_sel_hi:[1,0,0]\n\t"
        "v_fma_mix_f32 v4, v4, v128, v132 op_sel_hi:[1,0,0]\n\t"
        "v_fma_mix_f32 v8, v8, v128, v132 op_sel_hi:[1,0,0]\n\t"
        "buffer_load_dwordx4 v[32:35], v146, s[20:23], s93 offen\n\t"
        "v_fma_mix_f32 v12, v12, v128, v132 op_sel_hi:[1,0,0]\n\t"
        "v_fma_mix_f32 v16, v16, v128, v132 op_sel_hi:[1,0,0]\n\t"
        "v_fma_mix_f32 v20, v20, v128, v132 op_sel_hi:[1,0,0]\n\t"
        "v_fma_mix_f32 v24, v24, v128, v132 op_sel_hi:[1,0,0]\n\t"
        "v_fma_mix_f32 v28, v28, v128, v132 op_sel_hi:[1,0,0]\n\t"
        "v_cvt_pk_bf16_f32 v136, v0, v4\n\t"
        "v_cvt_pk_bf16_f32 v137, v8, v12\n\t"
        "v_cvt_pk_bf16_f32 v138, v16, v20\n\t"
        "s_add_u32 s58, s93, 18432\n\t"
        "buffer_load_dwordx4 v[36:39], v146, s[20:23], s58 offen\n\t"
        "v_cvt_pk_bf16_f32 v139, v24, v28\n\t"
        "ds_write_b128 v147, v[136:139] offset:0\n\t"
        "v_fma_mix_f32 v1, v1, v129, v133 op_sel_hi:[1,0,0]\n\t"
        "v_fma_mix_f32 v5, v5, v129, v133 op_sel_hi:[1,0,0]\n\t"
        "v_fma_mix_f32 v9, v9, v129, v133 op_sel_hi:[1,0,0]\n\t"
        "v_fma_mix_f32 v13, v13, v129, v133 op_sel_hi:[1,0,0]\n\t"
        "v_fma_mix_f32 v17, v17, v129, v133 op_sel_hi:[1,0,0]\n\t"
        "s_add_u32 s58, s93, 36864\n\t"
        "buffer_load_dwordx4 v[40:43], v146, s[20:23], s58 offen\n\t"
        "v_fma_mix_f32 v21, v21, v129, v133 op_sel_hi:[1,0,0]\n\t"
        "v_fma_mix_f32 v25, v25, v129, v133 op_sel_hi:[1,0,0]\n\t"
        "v_fma_mix_f32 v29, v29, v129, v133 op_sel_hi:[1,0,0]\n\t"
        "v_cvt_pk_bf16_f32 v140, v1, v5\n\t"
        "v_cvt_pk_bf16_f32 v141, v9, v13\n\t"
        "v_cvt_pk_bf16_f32 v142, v17, v21\n\t"
        "v_cvt_pk_bf16_f32 v143, v25, v29\n\t"
        "s_add_u32 s58, s93, 55296\n\t"
        "buffer_load_dwordx4 v[44:47], v146, s[20:23], s58 offen\n\t"
        "v_xor_b32 v164, s89, v147\n\t"
        "ds_write_b128 v164, v[140:143] offset:1024\n\t"
        "v_fma_mix_f32 v2, v2, v130, v134 op_sel_hi:[1,0,0]\n\t"
        "v_fma_mix_f32 v6, v6, v130, v134 op_sel_hi:[1,0,0]\n\t"
        "v_fma_mix_f32 v10, v10, v130, v134 op_sel_hi:[1,0,0]\n\t"
        "v_fma_mix_f32 v14, v14, v130, v134 op_sel_hi:[1,0,0]\n\t"
        "v_fma_mix_f32 v18, v18, v130, v134 op_sel_hi:[1,0,0]\n\t"
        "v_fma_mix_f32 v22, v22, v130, v134 op_sel_hi:[1,0,0]\n\t"
        "s_add_u32 s58, s93, 73728\n\t"
        "buffer_load_dwordx4 v[48:51], v146, s[20:23], s58 offen\n\t"
        "v_fma_mix_f32 v26, v26, v130, v134 op_sel_hi:[1,0,0]\n\t"
        "v_fma_mix_f32 v30, v30, v130, v134 op_sel_hi:[1,0,0]\n\t"
        "v_cvt_pk_bf16_f32 v136, v2, v6\n\t"
        "v_cvt_pk_bf16_f32 v137, v10, v14\n\t"
        "v_cvt_pk_bf16_f32 v138, v18, v22\n\t"
        "v_cvt_pk_bf16_f32 v139, v26, v30\n\t"
        "v_xor_b32 v164, s90, v147\n\t"
        "s_add_u32 s58, s93, 92160\n\t"
        "buffer_load_dwordx4 v[52:55], v146, s[20:23], s58 offen\n\t"
        "ds_write_b128 v164, v[136:139] offset:2048\n\t"
        "v_fma_mix_f32 v3, v3, v131, v135 op_sel_hi:[1,0,0]\n\t"
        "v_fma_mix_f32 v7, v7, v131, v135 op_sel_hi:[1,0,0]\n\t"
        "v_fma_mix_f32 v11, v11, v131, v135 op_sel_hi:[1,0,0]\n\t"
        "v_fma_mix_f32 v15, v15, v131, v135 op_sel_hi:[1,0,0]\n\t"
        "v_fma_mix_f32 v19, v19, v131, v135 op_sel_hi:[1,0,0]\n\t"
        "v_fma_mix_f32 v23, v23, v131, v135 op_sel_hi:[1,0,0]\n\t"
        "s_add_u32 s58, s93, 110592\n\t"
        "buffer_load_dwordx4 v[56:59], v146, s[20:23], s58 offen\n\t"
        "v_fma_mix_f32 v27, v27, v131, v135 op_sel_hi:[1,0,0]\n\t"
        "v_fma_mix_f32 v31, v31, v131, v135 op_sel_hi:[1,0,0]\n\t"
        "v_cvt_pk_bf16_f32 v140, v3, v7\n\t"
        "v_cvt_pk_bf16_f32 v141, v11, v15\n\t"
        "v_cvt_pk_bf16_f32 v142, v19, v23\n\t"
        "v_cvt_pk_bf16_f32 v143, v27, v31\n\t"
        "v_xor_b32 v164, s91, v147\n\t"
        "s_add_u32 s58, s93, 129024\n\t"
        "buffer_load_dwordx4 v[60:63], v146, s[20:23], s58 offen\n\t"
        "ds_write_b128 v164, v[140:143] offset:3072\n\t"
        "s_waitcnt lgkmcnt(0)\n\t"
        "s_barrier\n\t"
        "ds_read_b128 v[128:131], v151 offset:45056\n\t"
        "ds_read_b128 v[132:135], v151 offset:45824\n\t"
        "s_cmp_eq_u32 s101, 2\n\t"
        "s_cselect_b32 s89, 32, 0\n\t"
        "s_cselect_b32 s90, 64, 0\n\t"
        "s_cselect_b32 s91, 0x60, 0\n\t"
        "v_add_u32 v147, s97, v166\n\t"
        "s_lshr_b32 s86, 0x44042303, s88\n\t"
        "s_bfe_u32 s18, s86, 0x20005\n\t"
        "s_and_b32 s13, s86, 31\n\t"
        "s_lshr_b32 s86, s13, 1\n\t"
        "s_lshl_b32 s86, s86, 2\n\t"
        "s_lshr_b64 s[86:87], s[84:85], s86\n\t"
        "s_and_b32 s86, s86, 15\n\t"
        "s_lshl_b32 s86, s86, 1\n\t"
        "s_and_b32 s87, s13, 1\n\t"
        "s_or_b32 s86, s86, s87\n\t"
        "s_cmp_lt_u32 s13, 18\n\t"
        "s_cselect_b32 s86, s86, 64\n\t"
        "s_mul_i32 s94, s86, 589824\n\t"
        "s_lshr_b32 s86, 0x2010800, s88\n\t"
        "s_and_b32 s86, s86, 31\n\t"
        "s_lshl_b32 s98, s86, 12\n\t"
        "s_and_b32 s86, s18, 1\n\t"
        "s_lshl_b32 s86, s86, 8\n\t"
        "s_add_u32 s94, s94, s86\n\t"
        "s_cmp_eq_u32 s18, 2\n\t"
        "s_cselect_b64 vcc, -1, 0\n\t"
        "v_cndmask_b32 v146, v144, v145, vcc\n\t"
        "v_cndmask_b32 v152, v148, v149, vcc\n\t"
        "v_add_u32 v152, s86, v152\n\t"
        "buffer_load_dwordx4 v[64:67], v146, s[20:23], s94 offen\n\t"
        "s_add_u32 s58, s94, 18432\n\t"
        "buffer_load_dwordx4 v[68:71], v146, s[20:23], s58 offen\n\t"
        "s_add_u32 s58, s94, 36864\n\t"
        "buffer_load_dwordx4 v[72:75], v146, s[20:23], s58 offen\n\t"
        "s_add_u32 s58, s94, 55296\n\t"
        "buffer_load_dwordx4 v[76:79], v146, s[20:23], s58 offen\n\t"
        "s_add_u32 s58, s94, 73728\n\t"
        "buffer_load_dwordx4 v[80:83], v146, s[20:23], s58 offen\n\t"
        "s_add_u32 s58, s94, 92160\n\t"
        "buffer_load_dwordx4 v[84:87], v146, s[20:23], s58 offen\n\t"
        "s_add_u32 s58, s94, 110592\n\t"
        "buffer_load_dwordx4 v[88:91], v146, s[20:23], s58 offen\n\t"
        "s_add_u32 s58, s94, 129024\n\t"
        "buffer_load_dwordx4 v[92:95], v146, s[20:23], s58 offen\n\t"
        "s_waitcnt vmcnt(15) lgkmcnt(0)\n\t"
        "v_fma_mix_f32 v32, v32, v128, v132 op_sel_hi:[1,0,0]\n\t"
        "s_waitcnt vmcnt(14)\n\t"
        "v_fma_mix_f32 v36, v36, v128, v132 op_sel_hi:[1,0,0]\n\t"
        "s_waitcnt vmcnt(13)\n\t"
        "v_fma_mix_f32 v40, v40, v128, v132 op_sel_hi:[1,0,0]\n\t"
        "s_waitcnt vmcnt(12)\n\t"
        "v_fma_mix_f32 v44, v44, v128, v132 op_sel_hi:[1,0,0]\n\t"
        "s_waitcnt vmcnt(11)\n\t"
        "v_fma_mix_f32 v48, v48, v128, v132 op_sel_hi:[1,0,0]\n\t"
        "s_waitcnt vmcnt(10)\n\t"
        "v_fma_mix_f32 v52, v52, v128, v132 op_sel_hi:[1,0,0]\n\t"
        "s_waitcnt vmcnt(9)\n\t"
        "v_fma_mix_f32 v56, v56, v128, v132 op_sel_hi:[1,0,0]\n\t"
        "s_waitcnt vmcnt(8)\n\t"
        "v_fma_mix_f32 v60, v60, v128, v132 op_sel_hi:[1,0,0]\n\t"
        "v_cvt_pk_bf16_f32 v136, v32, v36\n\t"
        "v_cvt_pk_bf16_f32 v137, v40, v44\n\t"
        "v_cvt_pk_bf16_f32 v138, v48, v52\n\t"
        "v_cvt_pk_bf16_f32 v139, v56, v60\n\t"
        "ds_write_b128 v147, v[136:139] offset:0\n\t"
        "s_waitcnt lgkmcnt(0)\n\t"
        "s_barrier\n\t"
        "v_fma_mix_f32 v33, v33, v129, v133 op_sel_hi:[1,0,0]\n\t"
        "v_fma_mix_f32 v37, v37, v129, v133 op_sel_hi:[1,0,0]\n\t"
        "v_fma_mix_f32 v41, v41, v129, v133 op_sel_hi:[1,0,0]\n\t"
        "v_fma_mix_f32 v45, v45, v129, v133 op_sel_hi:[1,0,0]\n\t"
        "v_fma_mix_f32 v49, v49, v129, v133 op_sel_hi:[1,0,0]\n\t"
        "v_fma_mix_f32 v53, v53, v129, v133 op_sel_hi:[1,0,0]\n\t"
        "v_fma_mix_f32 v57, v57, v129, v133 op_sel_hi:[1,0,0]\n\t"
        "v_fma_mix_f32 v61, v61, v129, v133 op_sel_hi:[1,0,0]\n\t"
        "v_cvt_pk_bf16_f32 v140, v33, v37\n\t"
        "v_cvt_pk_bf16_f32 v141, v41, v45\n\t"
        "v_cvt_pk_bf16_f32 v142, v49, v53\n\t"
        "v_cvt_pk_bf16_f32 v143, v57, v61\n\t"
        "v_xor_b32 v164, s89, v147\n\t"
        "ds_write_b128 v164, v[140:143] offset:1024\n\t"
        "v_fma_mix_f32 v34, v34, v130, v134 op_sel_hi:[1,0,0]\n\t"
        "v_fma_mix_f32 v38, v38, v130, v134 op_sel_hi:[1,0,0]\n\t"
        "v_fma_mix_f32 v42, v42, v130, v134 op_sel_hi:[1,0,0]\n\t"
        "v_fma_mix_f32 v46, v46, v130, v134 op_sel_hi:[1,0,0]\n\t"
        "v_fma_mix_f32 v50, v50, v130, v134 op_sel_hi:[1,0,0]\n\t"
        "v_fma_mix_f32 v54, v54, v130, v134 op_sel_hi:[1,0,0]\n\t"
        "v_fma_mix_f32 v58, v58, v130, v134 op_sel_hi:[1,0,0]\n\t"
        "v_fma_mix_f32 v62, v62, v130, v134 op_sel_hi:[1,0,0]\n\t"
        "v_cvt_pk_bf16_f32 v136, v34, v38\n\t"
        "v_cvt_pk_bf16_f32 v137, v42, v46\n\t"
        "v_cvt_pk_bf16_f32 v138, v50, v54\n\t"
        "v_cvt_pk_bf16_f32 v139, v58, v62\n\t"
        "v_xor_b32 v164, s90, v147\n\t"
        "ds_write_b128 v164, v[136:139] offset:2048\n\t"
        "v_fma_mix_f32 v35, v35, v131, v135 op_sel_hi:[1,0,0]\n\t"
        "v_fma_mix_f32 v39, v39, v131, v135 op_sel_hi:[1,0,0]\n\t"
        "v_fma_mix_f32 v43, v43, v131, v135 op_sel_hi:[1,0,0]\n\t"
        "v_fma_mix_f32 v47, v47, v131, v135 op_sel_hi:[1,0,0]\n\t"
        "v_fma_mix_f32 v51, v51, v131, v135 op_sel_hi:[1,0,0]\n\t"
        "v_fma_mix_f32 v55, v55, v131, v135 op_sel_hi:[1,0,0]\n\t"
        "v_fma_mix_f32 v59, v59, v131, v135 op_sel_hi:[1,0,0]\n\t"
        "v_fma_mix_f32 v63, v63, v131, v135 op_sel_hi:[1,0,0]\n\t"
        "v_cvt_pk_bf16_f32 v140, v35, v39\n\t"
        "v_cvt_pk_bf16_f32 v141, v43, v47\n\t"
        "v_cvt_pk_bf16_f32 v142, v51, v55\n\t"
        "v_cvt_pk_bf16_f32 v143, v59, v63\n\t"
        "v_xor_b32 v164, s91, v147\n\t"
        "ds_write_b128 v164, v[140:143] offset:3072\n\t"
        "s_waitcnt lgkmcnt(0)\n\t"
        "s_barrier\n\t"
        "ds_read_b128 v[128:131], v152 offset:45056\n\t"
        "ds_read_b128 v[132:135], v152 offset:45824\n\t"
        "s_cmp_eq_u32 s18, 2\n\t"
        "s_cselect_b32 s89, 32, 0\n\t"
        "s_cselect_b32 s90, 64, 0\n\t"
        "s_cselect_b32 s91, 0x60, 0\n\t"
        "v_add_u32 v147, s98, v166\n\t"
        "s_lshr_b32 s86, 0x6250524, s88\n\t"
        "s_bfe_u32 s19, s86, 0x20005\n\t"
        "s_and_b32 s13, s86, 31\n\t"
        "s_lshr_b32 s86, s13, 1\n\t"
        "s_lshl_b32 s86, s86, 2\n\t"
        "s_lshr_b64 s[86:87], s[84:85], s86\n\t"
        "s_and_b32 s86, s86, 15\n\t"
        "s_lshl_b32 s86, s86, 1\n\t"
        "s_and_b32 s87, s13, 1\n\t"
        "s_or_b32 s86, s86, s87\n\t"
        "s_cmp_lt_u32 s13, 18\n\t"
        "s_cselect_b32 s86, s86, 64\n\t"
        "s_mul_i32 s95, s86, 589824\n\t"
        "s_lshr_b32 s86, 0x7050403, s88\n\t"
        "s_and_b32 s86, s86, 31\n\t"
        "s_lshl_b32 s99, s86, 12\n\t"
        "s_and_b32 s86, s19, 1\n\t"
        "s_lshl_b32 s86, s86, 8\n\t"
        "s_add_u32 s95, s95, s86\n\t"
        "s_cmp_eq_u32 s19, 2\n\t"
        "s_cselect_b64 vcc, -1, 0\n\t"
        "v_cndmask_b32 v146, v144, v145, vcc\n\t"
        "v_cndmask_b32 v153, v148, v149, vcc\n\t"
        "v_add_u32 v153, s86, v153\n\t"
        "buffer_load_dwordx4 v[96:99], v146, s[20:23], s95 offen\n\t"
        "s_add_u32 s58, s95, 18432\n\t"
        "buffer_load_dwordx4 v[100:103], v146, s[20:23], s58 offen\n\t"
        "s_add_u32 s58, s95, 36864\n\t"
        "buffer_load_dwordx4 v[104:107], v146, s[20:23], s58 offen\n\t"
        "s_add_u32 s58, s95, 55296\n\t"
        "buffer_load_dwordx4 v[108:111], v146, s[20:23], s58 offen\n\t"
        "s_add_u32 s58, s95, 73728\n\t"
        "buffer_load_dwordx4 v[112:115], v146, s[20:23], s58 offen\n\t"
        "s_add_u32 s58, s95, 92160\n\t"
        "buffer_load_dwordx4 v[116:119], v146, s[20:23], s58 offen\n\t"
        "s_add_u32 s58, s95, 110592\n\t"
        "buffer_load_dwordx4 v[120:123], v146, s[20:23], s58 offen\n\t"
        "s_add_u32 s58, s95, 129024\n\t"
        "buffer_load_dwordx4 v[124:127], v146, s[20:23], s58 offen\n\t"
        "s_waitcnt vmcnt(15) lgkmcnt(0)\n\t"
        "v_fma_mix_f32 v64, v64, v128, v132 op_sel_hi:[1,0,0]\n\t"
        "s_waitcnt vmcnt(14)\n\t"
        "v_fma_mix_f32 v68, v68, v128, v132 op_sel_hi:[1,0,0]\n\t"
        "s_waitcnt vmcnt(13)\n\t"
        "v_fma_mix_f32 v72, v72, v128, v132 op_sel_hi:[1,0,0]\n\t"
        "s_waitcnt vmcnt(12)\n\t"
        "v_fma_mix_f32 v76, v76, v128, v132 op_sel_hi:[1,0,0]\n\t"
        "s_waitcnt vmcnt(11)\n\t"
        "v_fma_mix_f32 v80, v80, v128, v132 op_sel_hi:[1,0,0]\n\t"
        "s_waitcnt vmcnt(10)\n\t"
        "v_fma_mix_f32 v84, v84, v128, v132 op_sel_hi:[1,0,0]\n\t"
        "s_waitcnt vmcnt(9)\n\t"
        "v_fma_mix_f32 v88, v88, v128, v132 op_sel_hi:[1,0,0]\n\t"
        "s_waitcnt vmcnt(8)\n\t"
        "v_fma_mix_f32 v92, v92, v128, v132 op_sel_hi:[1,0,0]\n\t"
        "v_cvt_pk_bf16_f32 v136, v64, v68\n\t"
        "v_cvt_pk_bf16_f32 v137, v72, v76\n\t"
        "v_cvt_pk_bf16_f32 v138, v80, v84\n\t"
        "v_cvt_pk_bf16_f32 v139, v88, v92\n\t"
        "ds_write_b128 v147, v[136:139] offset:0\n\t"
        "s_waitcnt lgkmcnt(0)\n\t"
        "s_barrier\n\t"
        "v_fma_mix_f32 v65, v65, v129, v133 op_sel_hi:[1,0,0]\n\t"
        "v_fma_mix_f32 v69, v69, v129, v133 op_sel_hi:[1,0,0]\n\t"
        "v_fma_mix_f32 v73, v73, v129, v133 op_sel_hi:[1,0,0]\n\t"
        "v_fma_mix_f32 v77, v77, v129, v133 op_sel_hi:[1,0,0]\n\t"
        "v_fma_mix_f32 v81, v81, v129, v133 op_sel_hi:[1,0,0]\n\t"
        "v_fma_mix_f32 v85, v85, v129, v133 op_sel_hi:[1,0,0]\n\t"
        "v_fma_mix_f32 v89, v89, v129, v133 op_sel_hi:[1,0,0]\n\t"
        "v_fma_mix_f32 v93, v93, v129, v133 op_sel_hi:[1,0,0]\n\t"
        "v_cvt_pk_bf16_f32 v140, v65, v69\n\t"
        "v_cvt_pk_bf16_f32 v141, v73, v77\n\t"
        "v_cvt_pk_bf16_f32 v142, v81, v85\n\t"
        "v_cvt_pk_bf16_f32 v143, v89, v93\n\t"
        "v_xor_b32 v164, s89, v147\n\t"
        "ds_write_b128 v164, v[140:143] offset:1024\n\t"
        "v_fma_mix_f32 v66, v66, v130, v134 op_sel_hi:[1,0,0]\n\t"
        "v_fma_mix_f32 v70, v70, v130, v134 op_sel_hi:[1,0,0]\n\t"
        "v_fma_mix_f32 v74, v74, v130, v134 op_sel_hi:[1,0,0]\n\t"
        "v_fma_mix_f32 v78, v78, v130, v134 op_sel_hi:[1,0,0]\n\t"
        "v_fma_mix_f32 v82, v82, v130, v134 op_sel_hi:[1,0,0]\n\t"
        "v_fma_mix_f32 v86, v86, v130, v134 op_sel_hi:[1,0,0]\n\t"
        "v_fma_mix_f32 v90, v90, v130, v134 op_sel_hi:[1,0,0]\n\t"
        "v_fma_mix_f32 v94, v94, v130, v134 op_sel_hi:[1,0,0]\n\t"
        "v_cvt_pk_bf16_f32 v136, v66, v70\n\t"
        "v_cvt_pk_bf16_f32 v137, v74, v78\n\t"
        "v_cvt_pk_bf16_f32 v138, v82, v86\n\t"
        "v_cvt_pk_bf16_f32 v139, v90, v94\n\t"
        "v_xor_b32 v164, s90, v147\n\t"
        "ds_write_b128 v164, v[136:139] offset:2048\n\t"
        "v_fma_mix_f32 v67, v67, v131, v135 op_sel_hi:[1,0,0]\n\t"
        "v_fma_mix_f32 v71, v71, v131, v135 op_sel_hi:[1,0,0]\n\t"
        "v_fma_mix_f32 v75, v75, v131, v135 op_sel_hi:[1,0,0]\n\t"
        "v_fma_mix_f32 v79, v79, v131, v135 op_sel_hi:[1,0,0]\n\t"
        "v_fma_mix_f32 v83, v83, v131, v135 op_sel_hi:[1,0,0]\n\t"
        "v_fma_mix_f32 v87, v87, v131, v135 op_sel_hi:[1,0,0]\n\t"
        "v_fma_mix_f32 v91, v91, v131, v135 op_sel_hi:[1,0,0]\n\t"
        "v_fma_mix_f32 v95, v95, v131, v135 op_sel_hi:[1,0,0]\n\t"
        "v_cvt_pk_bf16_f32 v140, v67, v71\n\t"
        "v_cvt_pk_bf16_f32 v141, v75, v79\n\t"
        "v_cvt_pk_bf16_f32 v142, v83, v87\n\t"
        "v_cvt_pk_bf16_f32 v143, v91, v95\n\t"
        "v_xor_b32 v164, s91, v147\n\t"
        "ds_write_b128 v164, v[140:143] offset:3072\n\t"
        "s_waitcnt lgkmcnt(0)\n\t"
        "s_barrier\n\t"
        "ds_read_b128 v[128:131], v153 offset:45056\n\t"
        "ds_read_b128 v[132:135], v153 offset:45824\n\t"
        "s_cmp_eq_u32 s19, 2\n\t"
        "s_cselect_b32 s89, 32, 0\n\t"
        "s_cselect_b32 s90, 64, 0\n\t"
        "s_cselect_b32 s91, 0x60, 0\n\t"
        "v_add_u32 v147, s99, v166\n\t"
        "s_lshr_b32 s86, 0x27072646, s88\n\t"
        "s_bfe_u32 s100, s86, 0x20005\n\t"
        "s_and_b32 s13, s86, 31\n\t"
        "s_lshr_b32 s86, s13, 1\n\t"
        "s_lshl_b32 s86, s86, 2\n\t"
        "s_lshr_b64 s[86:87], s[84:85], s86\n\t"
        "s_and_b32 s86, s86, 15\n\t"
        "s_lshl_b32 s86, s86, 1\n\t"
        "s_and_b32 s87, s13, 1\n\t"
        "s_or_b32 s86, s86, s87\n\t"
        "s_cmp_lt_u32 s13, 18\n\t"
        "s_cselect_b32 s86, s86, 64\n\t"
        "s_mul_i32 s92, s86, 589824\n\t"
        "s_lshr_b32 s86, 0x8010600, s88\n\t"
        "s_and_b32 s86, s86, 31\n\t"
        "s_lshl_b32 s96, s86, 12\n\t"
        "s_and_b32 s86, s100, 1\n\t"
        "s_lshl_b32 s86, s86, 8\n\t"
        "s_add_u32 s92, s92, s86\n\t"
        "s_cmp_eq_u32 s100, 2\n\t"
        "s_cselect_b64 vcc, -1, 0\n\t"
        "v_cndmask_b32 v146, v144, v145, vcc\n\t"
        "v_cndmask_b32 v150, v148, v149, vcc\n\t"
        "v_add_u32 v150, s86, v150\n\t"
        "buffer_load_dwordx4 v[0:3], v146, s[20:23], s92 offen\n\t"
        "s_add_u32 s58, s92, 18432\n\t"
        "buffer_load_dwordx4 v[4:7], v146, s[20:23], s58 offen\n\t"
        "s_add_u32 s58, s92, 36864\n\t"
        "buffer_load_dwordx4 v[8:11], v146, s[20:23], s58 offen\n\t"
        "s_add_u32 s58, s92, 55296\n\t"
        "buffer_load_dwordx4 v[12:15], v146, s[20:23], s58 offen\n\t"
        "s_add_u32 s58, s92, 73728\n\t"
        "buffer_load_dwordx4 v[16:19], v146, s[20:23], s58 offen\n\t"
        "s_add_u32 s58, s92, 92160\n\t"
        "buffer_load_dwordx4 v[20:23], v146, s[20:23], s58 offen\n\t"
        "s_add_u32 s58, s92, 110592\n\t"
        "buffer_load_dwordx4 v[24:27], v146, s[20:23], s58 offen\n\t"
        "s_add_u32 s58, s92, 129024\n\t"
        "buffer_load_dwordx4 v[28:31], v146, s[20:23], s58 offen\n\t"
        "s_waitcnt vmcnt(15) lgkmcnt(0)\n\t"
        "v_fma_mix_f32 v96, v96, v128, v132 op_sel_hi:[1,0,0]\n\t"
        "s_waitcnt vmcnt(14)\n\t"
        "v_fma_mix_f32 v100, v100, v128, v132 op_sel_hi:[1,0,0]\n\t"
        "s_waitcnt vmcnt(13)\n\t"
        "v_fma_mix_f32 v104, v104, v128, v132 op_sel_hi:[1,0,0]\n\t"
        "s_waitcnt vmcnt(12)\n\t"
        "v_fma_mix_f32 v108, v108, v128, v132 op_sel_hi:[1,0,0]\n\t"
        "s_waitcnt vmcnt(11)\n\t"
        "v_fma_mix_f32 v112, v112, v128, v132 op_sel_hi:[1,0,0]\n\t"
        "s_waitcnt vmcnt(10)\n\t"
        "v_fma_mix_f32 v116, v116, v128, v132 op_sel_hi:[1,0,0]\n\t"
        "s_waitcnt vmcnt(9)\n\t"
        "v_fma_mix_f32 v120, v120, v128, v132 op_sel_hi:[1,0,0]\n\t"
        "s_waitcnt vmcnt(8)\n\t"
        "v_fma_mix_f32 v124, v124, v128, v132 op_sel_hi:[1,0,0]\n\t"
        "v_cvt_pk_bf16_f32 v136, v96, v100\n\t"
        "v_cvt_pk_bf16_f32 v137, v104, v108\n\t"
        "v_cvt_pk_bf16_f32 v138, v112, v116\n\t"
        "v_cvt_pk_bf16_f32 v139, v120, v124\n\t"
        "ds_write_b128 v147, v[136:139] offset:0\n\t"
        "s_waitcnt lgkmcnt(0)\n\t"
        "s_barrier\n\t"
        "v_fma_mix_f32 v97, v97, v129, v133 op_sel_hi:[1,0,0]\n\t"
        "v_fma_mix_f32 v101, v101, v129, v133 op_sel_hi:[1,0,0]\n\t"
        "v_fma_mix_f32 v105, v105, v129, v133 op_sel_hi:[1,0,0]\n\t"
        "v_fma_mix_f32 v109, v109, v129, v133 op_sel_hi:[1,0,0]\n\t"
        "v_fma_mix_f32 v113, v113, v129, v133 op_sel_hi:[1,0,0]\n\t"
        "v_fma_mix_f32 v117, v117, v129, v133 op_sel_hi:[1,0,0]\n\t"
        "v_fma_mix_f32 v121, v121, v129, v133 op_sel_hi:[1,0,0]\n\t"
        "v_fma_mix_f32 v125, v125, v129, v133 op_sel_hi:[1,0,0]\n\t"
        "v_cvt_pk_bf16_f32 v140, v97, v101\n\t"
        "v_cvt_pk_bf16_f32 v141, v105, v109\n\t"
        "v_cvt_pk_bf16_f32 v142, v113, v117\n\t"
        "v_cvt_pk_bf16_f32 v143, v121, v125\n\t"
        "v_xor_b32 v164, s89, v147\n\t"
        "ds_write_b128 v164, v[140:143] offset:1024\n\t"
        "v_fma_mix_f32 v98, v98, v130, v134 op_sel_hi:[1,0,0]\n\t"
        "v_fma_mix_f32 v102, v102, v130, v134 op_sel_hi:[1,0,0]\n\t"
        "v_fma_mix_f32 v106, v106, v130, v134 op_sel_hi:[1,0,0]\n\t"
        "v_fma_mix_f32 v110, v110, v130, v134 op_sel_hi:[1,0,0]\n\t"
        "v_fma_mix_f32 v114, v114, v130, v134 op_sel_hi:[1,0,0]\n\t"
        "v_fma_mix_f32 v118, v118, v130, v134 op_sel_hi:[1,0,0]\n\t"
        "v_fma_mix_f32 v122, v122, v130, v134 op_sel_hi:[1,0,0]\n\t"
        "v_fma_mix_f32 v126, v126, v130, v134 op_sel_hi:[1,0,0]\n\t"
        "v_cvt_pk_bf16_f32 v136, v98, v102\n\t"
        "v_cvt_pk_bf16_f32 v137, v106, v110\n\t"
        "v_cvt_pk_bf16_f32 v138, v114, v118\n\t"
        "v_cvt_pk_bf16_f32 v139, v122, v126\n\t"
        "v_xor_b32 v164, s90, v147\n\t"
        "ds_write_b128 v164, v[136:139] offset:2048\n\t"
        "v_fma_mix_f32 v99, v99, v131, v135 op_sel_hi:[1,0,0]\n\t"
        "v_fma_mix_f32 v103, v103, v131, v135 op_sel_hi:[1,0,0]\n\t"
        "v_fma_mix_f32 v107, v107, v131, v135 op_sel_hi:[1,0,0]\n\t"
        "v_fma_mix_f32 v111, v111, v131, v135 op_sel_hi:[1,0,0]\n\t"
        "v_fma_mix_f32 v115, v115, v131, v135 op_sel_hi:[1,0,0]\n\t"
        "v_fma_mix_f32 v119, v119, v131, v135 op_sel_hi:[1,0,0]\n\t"
        "v_fma_mix_f32 v123, v123, v131, v135 op_sel_hi:[1,0,0]\n\t"
        "v_fma_mix_f32 v127, v127, v131, v135 op_sel_hi:[1,0,0]\n\t"
        "v_cvt_pk_bf16_f32 v140, v99, v103\n\t"
        "v_cvt_pk_bf16_f32 v141, v107, v111\n\t"
        "v_cvt_pk_bf16_f32 v142, v115, v119\n\t"
        "v_cvt_pk_bf16_f32 v143, v123, v127\n\t"
        "v_xor_b32 v164, s91, v147\n\t"
        "ds_write_b128 v164, v[140:143] offset:3072\n\t"
        "s_waitcnt lgkmcnt(0)\n\t"
        "s_barrier\n\t"
        "ds_read_b128 v[128:131], v150 offset:45056\n\t"
        "ds_read_b128 v[132:135], v150 offset:45824\n\t"
        "s_cmp_eq_u32 s100, 2\n\t"
        "s_cselect_b32 s89, 32, 0\n\t"
        "s_cselect_b32 s90, 64, 0\n\t"
        "s_cselect_b32 s91, 0x60, 0\n\t"
        "v_add_u32 v147, s96, v166\n\t"
        "s_lshr_b32 s86, 0x9284808, s88\n\t"
        "s_bfe_u32 s101, s86, 0x20005\n\t"
        "s_and_b32 s13, s86, 31\n\t"
        "s_lshr_b32 s86, s13, 1\n\t"
        "s_lshl_b32 s86, s86, 2\n\t"
        "s_lshr_b64 s[86:87], s[84:85], s86\n\t"
        "s_and_b32 s86, s86, 15\n\t"
        "s_lshl_b32 s86, s86, 1\n\t"
        "s_and_b32 s87, s13, 1\n\t"
        "s_or_b32 s86, s86, s87\n\t"
        "s_cmp_lt_u32 s13, 18\n\t"
        "s_cselect_b32 s86, s86, 64\n\t"
        "s_mul_i32 s93, s86, 589824\n\t"
        "s_lshr_b32 s86, 0x5040302, s88\n\t"
        "s_and_b32 s86, s86, 31\n\t"
        "s_lshl_b32 s97, s86, 12\n\t"
        "s_and_b32 s86, s101, 1\n\t"
        "s_lshl_b32 s86, s86, 8\n\t"
        "s_add_u32 s93, s93, s86\n\t"
        "s_cmp_eq_u32 s101, 2\n\t"
        "s_cselect_b64 vcc, -1, 0\n\t"
        "v_cndmask_b32 v146, v144, v145, vcc\n\t"
        "v_cndmask_b32 v151, v148, v149, vcc\n\t"
        "v_add_u32 v151, s86, v151\n\t"
        "buffer_load_dwordx4 v[32:35], v146, s[20:23], s93 offen\n\t"
        "s_add_u32 s58, s93, 18432\n\t"
        "buffer_load_dwordx4 v[36:39], v146, s[20:23], s58 offen\n\t"
        "s_add_u32 s58, s93, 36864\n\t"
        "buffer_load_dwordx4 v[40:43], v146, s[20:23], s58 offen\n\t"
        "s_add_u32 s58, s93, 55296\n\t"
        "buffer_load_dwordx4 v[44:47], v146, s[20:23], s58 offen\n\t"
        "s_add_u32 s58, s93, 73728\n\t"
        "buffer_load_dwordx4 v[48:51], v146, s[20:23], s58 offen\n\t"
        "s_add_u32 s58, s93, 92160\n\t"
        "buffer_load_dwordx4 v[52:55], v146, s[20:23], s58 offen\n\t"
        "s_add_u32 s58, s93, 110592\n\t"
        "buffer_load_dwordx4 v[56:59], v146, s[20:23], s58 offen\n\t"
        "s_add_u32 s58, s93, 129024\n\t"
        "buffer_load_dwordx4 v[60:63], v146, s[20:23], s58 offen\n\t"
        "s_waitcnt vmcnt(15) lgkmcnt(0)\n\t"
        "v_fma_mix_f32 v0, v0, v128, v132 op_sel_hi:[1,0,0]\n\t"
        "s_waitcnt vmcnt(14)\n\t"
        "v_fma_mix_f32 v4, v4, v128, v132 op_sel_hi:[1,0,0]\n\t"
        "s_waitcnt vmcnt(13)\n\t"
        "v_fma_mix_f32 v8, v8, v128, v132 op_sel_hi:[1,0,0]\n\t"
        "s_waitcnt vmcnt(12)\n\t"
        "v_fma_mix_f32 v12, v12, v128, v132 op_sel_hi:[1,0,0]\n\t"
        "s_waitcnt vmcnt(11)\n\t"
        "v_fma_mix_f32 v16, v16, v128, v132 op_sel_hi:[1,0,0]\n\t"
        "s_waitcnt vmcnt(10)\n\t"
        "v_fma_mix_f32 v20, v20, v128, v132 op_sel_hi:[1,0,0]\n\t"
        "s_waitcnt vmcnt(9)\n\t"
        "v_fma_mix_f32 v24, v24, v128, v132 op_sel_hi:[1,0,0]\n\t"
        "s_waitcnt vmcnt(8)\n\t"
        "v_fma_mix_f32 v28, v28, v128, v132 op_sel_hi:[1,0,0]\n\t"
        "v_cvt_pk_bf16_f32 v136, v0, v4\n\t"
        "v_cvt_pk_bf16_f32 v137, v8, v12\n\t"
        "v_cvt_pk_bf16_f32 v138, v16, v20\n\t"
        "v_cvt_pk_bf16_f32 v139, v24, v28\n\t"
        "ds_write_b128 v147, v[136:139] offset:0\n\t"
        "s_waitcnt lgkmcnt(0)\n\t"
        "s_barrier\n\t"
        "v_fma_mix_f32 v1, v1, v129, v133 op_sel_hi:[1,0,0]\n\t"
        "v_fma_mix_f32 v5, v5, v129, v133 op_sel_hi:[1,0,0]\n\t"
        "v_fma_mix_f32 v9, v9, v129, v133 op_sel_hi:[1,0,0]\n\t"
        "v_fma_mix_f32 v13, v13, v129, v133 op_sel_hi:[1,0,0]\n\t"
        "v_fma_mix_f32 v17, v17, v129, v133 op_sel_hi:[1,0,0]\n\t"
        "v_fma_mix_f32 v21, v21, v129, v133 op_sel_hi:[1,0,0]\n\t"
        "v_fma_mix_f32 v25, v25, v129, v133 op_sel_hi:[1,0,0]\n\t"
        "v_fma_mix_f32 v29, v29, v129, v133 op_sel_hi:[1,0,0]\n\t"
        "v_cvt_pk_bf16_f32 v140, v1, v5\n\t"
        "v_cvt_pk_bf16_f32 v141, v9, v13\n\t"
        "v_cvt_pk_bf16_f32 v142, v17, v21\n\t"
        "v_cvt_pk_bf16_f32 v143, v25, v29\n\t"
        "v_xor_b32 v164, s89, v147\n\t"
        "ds_write_b128 v164, v[140:143] offset:1024\n\t"
        "v_fma_mix_f32 v2, v2, v130, v134 op_sel_hi:[1,0,0]\n\t"
        "v_fma_mix_f32 v6, v6, v130, v134 op_sel_hi:[1,0,0]\n\t"
        "v_fma_mix_f32 v10, v10, v130, v134 op_sel_hi:[1,0,0]\n\t"
        "v_fma_mix_f32 v14, v14, v130, v134 op_sel_hi:[1,0,0]\n\t"
        "v_fma_mix_f32 v18, v18, v130, v134 op_sel_hi:[1,0,0]\n\t"
        "v_fma_mix_f32 v22, v22, v130, v134 op_sel_hi:[1,0,0]\n\t"
        "v_fma_mix_f32 v26, v26, v130, v134 op_sel_hi:[1,0,0]\n\t"
        "v_fma_mix_f32 v30, v30, v130, v134 op_sel_hi:[1,0,0]\n\t"
        "v_cvt_pk_bf16_f32 v136, v2, v6\n\t"
        "v_cvt_pk_bf16_f32 v137, v10, v14\n\t"
        "v_cvt_pk_bf16_f32 v138, v18, v22\n\t"
        "v_cvt_pk_bf16_f32 v139, v26, v30\n\t"
        "v_xor_b32 v164, s90, v147\n\t"
        "ds_write_b128 v164, v[136:139] offset:2048\n\t"
        "v_fma_mix_f32 v3, v3, v131, v135 op_sel_hi:[1,0,0]\n\t"
        "v_fma_mix_f32 v7, v7, v131, v135 op_sel_hi:[1,0,0]\n\t"
        "v_fma_mix_f32 v11, v11, v131, v135 op_sel_hi:[1,0,0]\n\t"
        "v_fma_mix_f32 v15, v15, v131, v135 op_sel_hi:[1,0,0]\n\t"
        "v_fma_mix_f32 v19, v19, v131, v135 op_sel_hi:[1,0,0]\n\t"
        "v_fma_mix_f32 v23, v23, v131, v135 op_sel_hi:[1,0,0]\n\t"
        "v_fma_mix_f32 v27, v27, v131, v135 op_sel_hi:[1,0,0]\n\t"
        "v_fma_mix_f32 v31, v31, v131, v135 op_sel_hi:[1,0,0]\n\t"
        "v_cvt_pk_bf16_f32 v140, v3, v7\n\t"
        "v_cvt_pk_bf16_f32 v141, v11, v15\n\t"
        "v_cvt_pk_bf16_f32 v142, v19, v23\n\t"
        "v_cvt_pk_bf16_f32 v143, v27, v31\n\t"
        "v_xor_b32 v164, s91, v147\n\t"
        "ds_write_b128 v164, v[140:143] offset:3072\n\t"
        "s_waitcnt lgkmcnt(0)\n\t"
        "s_barrier\n\t"
        "ds_read_b128 v[128:131], v151 offset:45056\n\t"
        "ds_read_b128 v[132:135], v151 offset:45824\n\t"
        "s_cmp_eq_u32 s101, 2\n\t"
        "s_cselect_b32 s89, 32, 0\n\t"
        "s_cselect_b32 s90, 64, 0\n\t"
        "s_cselect_b32 s91, 0x60, 0\n\t"
        "v_add_u32 v147, s97, v166\n\t"
        "s_lshr_b32 s86, 0xb4a0a29, s88\n\t"
        "s_bfe_u32 s18, s86, 0x20005\n\t"
        "s_and_b32 s13, s86, 31\n\t"
        "s_lshr_b32 s86, s13, 1\n\t"
        "s_lshl_b32 s86, s86, 2\n\t"
        "s_lshr_b64 s[86:87], s[84:85], s86\n\t"
        "s_and_b32 s86, s86, 15\n\t"
        "s_lshl_b32 s86, s86, 1\n\t"
        "s_and_b32 s87, s13, 1\n\t"
        "s_or_b32 s86, s86, s87\n\t"
        "s_cmp_lt_u32 s13, 18\n\t"
        "s_cselect_b32 s86, s86, 64\n\t"
        "s_mul_i32 s94, s86, 589824\n\t"
        "s_lshr_b32 s86, 0x1000706, s88\n\t"
        "s_and_b32 s86, s86, 31\n\t"
        "s_lshl_b32 s98, s86, 12\n\t"
        "s_and_b32 s86, s18, 1\n\t"
        "s_lshl_b32 s86, s86, 8\n\t"
        "s_add_u32 s94, s94, s86\n\t"
        "s_cmp_eq_u32 s18, 2\n\t"
        "s_cselect_b64 vcc, -1, 0\n\t"
        "v_cndmask_b32 v146, v144, v145, vcc\n\t"
        "v_cndmask_b32 v152, v148, v149, vcc\n\t"
        "v_add_u32 v152, s86, v152\n\t"
        "buffer_load_dwordx4 v[64:67], v146, s[20:23], s94 offen\n\t"
        "s_add_u32 s58, s94, 18432\n\t"
        "buffer_load_dwordx4 v[68:71], v146, s[20:23], s58 offen\n\t"
        "s_add_u32 s58, s94, 36864\n\t"
        "buffer_load_dwordx4 v[72:75], v146, s[20:23], s58 offen\n\t"
        "s_add_u32 s58, s94, 55296\n\t"
        "buffer_load_dwordx4 v[76:79], v146, s[20:23], s58 offen\n\t"
        "s_add_u32 s58, s94, 73728\n\t"
        "buffer_load_dwordx4 v[80:83], v146, s[20:23], s58 offen\n\t"
        "s_add_u32 s58, s94, 92160\n\t"
        "buffer_load_dwordx4 v[84:87], v146, s[20:23], s58 offen\n\t"
        "s_add_u32 s58, s94, 110592\n\t"
        "buffer_load_dwordx4 v[88:91], v146, s[20:23], s58 offen\n\t"
        "s_add_u32 s58, s94, 129024\n\t"
        "buffer_load_dwordx4 v[92:95], v146, s[20:23], s58 offen\n\t"
        "s_waitcnt vmcnt(15) lgkmcnt(0)\n\t"
        "v_fma_mix_f32 v32, v32, v128, v132 op_sel_hi:[1,0,0]\n\t"
        "s_waitcnt vmcnt(14)\n\t"
        "v_fma_mix_f32 v36, v36, v128, v132 op_sel_hi:[1,0,0]\n\t"
        "s_waitcnt vmcnt(13)\n\t"
        "v_fma_mix_f32 v40, v40, v128, v132 op_sel_hi:[1,0,0]\n\t"
        "s_waitcnt vmcnt(12)\n\t"
        "v_fma_mix_f32 v44, v44, v128, v132 op_sel_hi:[1,0,0]\n\t"
        "s_waitcnt vmcnt(11)\n\t"
        "v_fma_mix_f32 v48, v48, v128, v132 op_sel_hi:[1,0,0]\n\t"
        "s_waitcnt vmcnt(10)\n\t"
        "v_fma_mix_f32 v52, v52, v128, v132 op_sel_hi:[1,0,0]\n\t"
        "s_waitcnt vmcnt(9)\n\t"
        "v_fma_mix_f32 v56, v56, v128, v132 op_sel_hi:[1,0,0]\n\t"
        "s_waitcnt vmcnt(8)\n\t"
        "v_fma_mix_f32 v60, v60, v128, v132 op_sel_hi:[1,0,0]\n\t"
        "v_cvt_pk_bf16_f32 v136, v32, v36\n\t"
        "v_cvt_pk_bf16_f32 v137, v40, v44\n\t"
        "v_cvt_pk_bf16_f32 v138, v48, v52\n\t"
        "v_cvt_pk_bf16_f32 v139, v56, v60\n\t"
        "ds_write_b128 v147, v[136:139] offset:0\n\t"
        "s_waitcnt lgkmcnt(0)\n\t"
        "s_barrier\n\t"
        "v_fma_mix_f32 v33, v33, v129, v133 op_sel_hi:[1,0,0]\n\t"
        "v_fma_mix_f32 v37, v37, v129, v133 op_sel_hi:[1,0,0]\n\t"
        "v_fma_mix_f32 v41, v41, v129, v133 op_sel_hi:[1,0,0]\n\t"
        "v_fma_mix_f32 v45, v45, v129, v133 op_sel_hi:[1,0,0]\n\t"
        "v_fma_mix_f32 v49, v49, v129, v133 op_sel_hi:[1,0,0]\n\t"
        "v_fma_mix_f32 v53, v53, v129, v133 op_sel_hi:[1,0,0]\n\t"
        "v_fma_mix_f32 v57, v57, v129, v133 op_sel_hi:[1,0,0]\n\t"
        "v_fma_mix_f32 v61, v61, v129, v133 op_sel_hi:[1,0,0]\n\t"
        "v_cvt_pk_bf16_f32 v140, v33, v37\n\t"
        "v_cvt_pk_bf16_f32 v141, v41, v45\n\t"
        "v_cvt_pk_bf16_f32 v142, v49, v53\n\t"
        "v_cvt_pk_bf16_f32 v143, v57, v61\n\t"
        "v_xor_b32 v164, s89, v147\n\t"
        "ds_write_b128 v164, v[140:143] offset:1024\n\t"
        "v_fma_mix_f32 v34, v34, v130, v134 op_sel_hi:[1,0,0]\n\t"
        "v_fma_mix_f32 v38, v38, v130, v134 op_sel_hi:[1,0,0]\n\t"
        "v_fma_mix_f32 v42, v42, v130, v134 op_sel_hi:[1,0,0]\n\t"
        "v_fma_mix_f32 v46, v46, v130, v134 op_sel_hi:[1,0,0]\n\t"
        "v_fma_mix_f32 v50, v50, v130, v134 op_sel_hi:[1,0,0]\n\t"
        "v_fma_mix_f32 v54, v54, v130, v134 op_sel_hi:[1,0,0]\n\t"
        "v_fma_mix_f32 v58, v58, v130, v134 op_sel_hi:[1,0,0]\n\t"
        "v_fma_mix_f32 v62, v62, v130, v134 op_sel_hi:[1,0,0]\n\t"
        "v_cvt_pk_bf16_f32 v136, v34, v38\n\t"
        "v_cvt_pk_bf16_f32 v137, v42, v46\n\t"
        "v_cvt_pk_bf16_f32 v138, v50, v54\n\t"
        "v_cvt_pk_bf16_f32 v139, v58, v62\n\t"
        "v_xor_b32 v164, s90, v147\n\t"
        "ds_write_b128 v164, v[136:139] offset:2048\n\t"
        "v_fma_mix_f32 v35, v35, v131, v135 op_sel_hi:[1,0,0]\n\t"
        "v_fma_mix_f32 v39, v39, v131, v135 op_sel_hi:[1,0,0]\n\t"
        "v_fma_mix_f32 v43, v43, v131, v135 op_sel_hi:[1,0,0]\n\t"
        "v_fma_mix_f32 v47, v47, v131, v135 op_sel_hi:[1,0,0]\n\t"
        "v_fma_mix_f32 v51, v51, v131, v135 op_sel_hi:[1,0,0]\n\t"
        "v_fma_mix_f32 v55, v55, v131, v135 op_sel_hi:[1,0,0]\n\t"
        "v_fma_mix_f32 v59, v59, v131, v135 op_sel_hi:[1,0,0]\n\t"
        "v_fma_mix_f32 v63, v63, v131, v135 op_sel_hi:[1,0,0]\n\t"
        "v_cvt_pk_bf16_f32 v140, v35, v39\n\t"
        "v_cvt_pk_bf16_f32 v141, v43, v47\n\t"
        "v_cvt_pk_bf16_f32 v142, v51, v55\n\t"
        "v_cvt_pk_bf16_f32 v143, v59, v63\n\t"
        "v_xor_b32 v164, s91, v147\n\t"
        "ds_write_b128 v164, v[140:143] offset:3072\n\t"
        "s_waitcnt lgkmcnt(0)\n\t"
        "s_barrier\n\t"
        "ds_read_b128 v[128:131], v152 offset:45056\n\t"
        "ds_read_b128 v[132:135], v152 offset:45824\n\t"
        "s_cmp_eq_u32 s18, 2\n\t"
        "s_cselect_b32 s89, 32, 0\n\t"
        "s_cselect_b32 s90, 64, 0\n\t"
        "s_cselect_b32 s91, 0x60, 0\n\t"
        "v_add_u32 v147, s98, v166\n\t"
        "s_lshr_b32 s86, 0xe0d4c0c, s88\n\t"
        "s_bfe_u32 s19, s86, 0x20005\n\t"
        "s_and_b32 s13, s86, 31\n\t"
        "s_lshr_b32 s86, s13, 1\n\t"
        "s_lshl_b32 s86, s86, 2\n\t"
        "s_lshr_b64 s[86:87], s[84:85], s86\n\t"
        "s_and_b32 s86, s86, 15\n\t"
        "s_lshl_b32 s86, s86, 1\n\t"
        "s_and_b32 s87, s13, 1\n\t"
        "s_or_b32 s86, s86, s87\n\t"
        "s_cmp_lt_u32 s13, 18\n\t"
        "s_cselect_b32 s86, s86, 64\n\t"
        "s_mul_i32 s95, s86, 589824\n\t"
        "s_lshr_b32 s86, 0x4030802, s88\n\t"
        "s_and_b32 s86, s86, 31\n\t"
        "s_lshl_b32 s99, s86, 12\n\t"
        "s_and_b32 s86, s19, 1\n\t"
        "s_lshl_b32 s86, s86, 8\n\t"
        "s_add_u32 s95, s95, s86\n\t"
        "s_cmp_eq_u32 s19, 2\n\t"
        "s_cselect_b64 vcc, -1, 0\n\t"
        "v_cndmask_b32 v146, v144, v145, vcc\n\t"
        "v_cndmask_b32 v153, v148, v149, vcc\n\t"
        "v_add_u32 v153, s86, v153\n\t"
        "buffer_load_dwordx4 v[96:99], v146, s[20:23], s95 offen\n\t"
        "s_add_u32 s58, s95, 18432\n\t"
        "buffer_load_dwordx4 v[100:103], v146, s[20:23], s58 offen\n\t"
        "s_add_u32 s58, s95, 36864\n\t"
        "buffer_load_dwordx4 v[104:107], v146, s[20:23], s58 offen\n\t"
        "s_add_u32 s58, s95, 55296\n\t"
        "buffer_load_dwordx4 v[108:111], v146, s[20:23], s58 offen\n\t"
        "s_add_u32 s58, s95, 73728\n\t"
        "buffer_load_dwordx4 v[112:115], v146, s[20:23], s58 offen\n\t"
        "s_add_u32 s58, s95, 92160\n\t"
        "buffer_load_dwordx4 v[116:119], v146, s[20:23], s58 offen\n\t"
        "s_add_u32 s58, s95, 110592\n\t"
        "buffer_load_dwordx4 v[120:123], v146, s[20:23], s58 offen\n\t"
        "s_add_u32 s58, s95, 129024\n\t"
        "buffer_load_dwordx4 v[124:127], v146, s[20:23], s58 offen\n\t"
        "s_waitcnt vmcnt(15) lgkmcnt(0)\n\t"
        "v_fma_mix_f32 v64, v64, v128, v132 op_sel_hi:[1,0,0]\n\t"
        "s_waitcnt vmcnt(14)\n\t"
        "v_fma_mix_f32 v68, v68, v128, v132 op_sel_hi:[1,0,0]\n\t"
        "s_waitcnt vmcnt(13)\n\t"
        "v_fma_mix_f32 v72, v72, v128, v132 op_sel_hi:[1,0,0]\n\t"
        "s_waitcnt vmcnt(12)\n\t"
        "v_fma_mix_f32 v76, v76, v128, v132 op_sel_hi:[1,0,0]\n\t"
        "s_waitcnt vmcnt(11)\n\t"
        "v_fma_mix_f32 v80, v80, v128, v132 op_sel_hi:[1,0,0]\n\t"
        "s_waitcnt vmcnt(10)\n\t"
        "v_fma_mix_f32 v84, v84, v128, v132 op_sel_hi:[1,0,0]\n\t"
        "s_waitcnt vmcnt(9)\n\t"
        "v_fma_mix_f32 v88, v88, v128, v132 op_sel_hi:[1,0,0]\n\t"
        "s_waitcnt vmcnt(8)\n\t"
        "v_fma_mix_f32 v92, v92, v128, v132 op_sel_hi:[1,0,0]\n\t"
        "v_cvt_pk_bf16_f32 v136, v64, v68\n\t"
        "v_cvt_pk_bf16_f32 v137, v72, v76\n\t"
        "v_cvt_pk_bf16_f32 v138, v80, v84\n\t"
        "v_cvt_pk_bf16_f32 v139, v88, v92\n\t"
        "ds_write_b128 v147, v[136:139] offset:0\n\t"
        "s_waitcnt lgkmcnt(0)\n\t"
        "s_barrier\n\t"
        "v_fma_mix_f32 v65, v65, v129, v133 op_sel_hi:[1,0,0]\n\t"
        "v_fma_mix_f32 v69, v69, v129, v133 op_sel_hi:[1,0,0]\n\t"
        "v_fma_mix_f32 v73, v73, v129, v133 op_sel_hi:[1,0,0]\n\t"
        "v_fma_mix_f32 v77, v77, v129, v133 op_sel_hi:[1,0,0]\n\t"
        "v_fma_mix_f32 v81, v81, v129, v133 op_sel_hi:[1,0,0]\n\t"
        "v_fma_mix_f32 v85, v85, v129, v133 op_sel_hi:[1,0,0]\n\t"
        "v_fma_mix_f32 v89, v89, v129, v133 op_sel_hi:[1,0,0]\n\t"
        "v_fma_mix_f32 v93, v93, v129, v133 op_sel_hi:[1,0,0]\n\t"
        "v_cvt_pk_bf16_f32 v140, v65, v69\n\t"
        "v_cvt_pk_bf16_f32 v141, v73, v77\n\t"
        "v_cvt_pk_bf16_f32 v142, v81, v85\n\t"
        "v_cvt_pk_bf16_f32 v143, v89, v93\n\t"
        "v_xor_b32 v164, s89, v147\n\t"
        "ds_write_b128 v164, v[140:143] offset:1024\n\t"
        "v_fma_mix_f32 v66, v66, v130, v134 op_sel_hi:[1,0,0]\n\t"
        "v_fma_mix_f32 v70, v70, v130, v134 op_sel_hi:[1,0,0]\n\t"
        "v_fma_mix_f32 v74, v74, v130, v134 op_sel_hi:[1,0,0]\n\t"
        "v_fma_mix_f32 v78, v78, v130, v134 op_sel_hi:[1,0,0]\n\t"
        "v_fma_mix_f32 v82, v82, v130, v134 op_sel_hi:[1,0,0]\n\t"
        "v_fma_mix_f32 v86, v86, v130, v134 op_sel_hi:[1,0,0]\n\t"
        "v_fma_mix_f32 v90, v90, v130, v134 op_sel_hi:[1,0,0]\n\t"
        "v_fma_mix_f32 v94, v94, v130, v134 op_sel_hi:[1,0,0]\n\t"
        "v_cvt_pk_bf16_f32 v136, v66, v70\n\t"
        "v_cvt_pk_bf16_f32 v137, v74, v78\n\t"
        "v_cvt_pk_bf16_f32 v138, v82, v86\n\t"
        "v_cvt_pk_bf16_f32 v139, v90, v94\n\t"
        "v_xor_b32 v164, s90, v147\n\t"
        "ds_write_b128 v164, v[136:139] offset:2048\n\t"
        "v_fma_mix_f32 v67, v67, v131, v135 op_sel_hi:[1,0,0]\n\t"
        "v_fma_mix_f32 v71, v71, v131, v135 op_sel_hi:[1,0,0]\n\t"
        "v_fma_mix_f32 v75, v75, v131, v135 op_sel_hi:[1,0,0]\n\t"
        "v_fma_mix_f32 v79, v79, v131, v135 op_sel_hi:[1,0,0]\n\t"
        "v_fma_mix_f32 v83, v83, v131, v135 op_sel_hi:[1,0,0]\n\t"
        "v_fma_mix_f32 v87, v87, v131, v135 op_sel_hi:[1,0,0]\n\t"
        "v_fma_mix_f32 v91, v91, v131, v135 op_sel_hi:[1,0,0]\n\t"
        "v_fma_mix_f32 v95, v95, v131, v135 op_sel_hi:[1,0,0]\n\t"
        "v_cvt_pk_bf16_f32 v140, v67, v71\n\t"
        "v_cvt_pk_bf16_f32 v141, v75, v79\n\t"
        "v_cvt_pk_bf16_f32 v142, v83, v87\n\t"
        "v_cvt_pk_bf16_f32 v143, v91, v95\n\t"
        "v_xor_b32 v164, s91, v147\n\t"
        "ds_write_b128 v164, v[140:143] offset:3072\n\t"
        "s_waitcnt lgkmcnt(0)\n\t"
        "s_barrier\n\t"
        "ds_read_b128 v[128:131], v153 offset:45056\n\t"
        "ds_read_b128 v[132:135], v153 offset:45824\n\t"
        "s_cmp_eq_u32 s19, 2\n\t"
        "s_cselect_b32 s89, 32, 0\n\t"
        "s_cselect_b32 s90, 64, 0\n\t"
        "s_cselect_b32 s91, 0x60, 0\n\t"
        "v_add_u32 v147, s99, v166\n\t"
        "s_lshr_b32 s86, 0x50100f4e, s88\n\t"
        "s_bfe_u32 s100, s86, 0x20005\n\t"
        "s_and_b32 s13, s86, 31\n\t"
        "s_lshr_b32 s86, s13, 1\n\t"
        "s_lshl_b32 s86, s86, 2\n\t"
        "s_lshr_b64 s[86:87], s[84:85], s86\n\t"
        "s_and_b32 s86, s86, 15\n\t"
        "s_lshl_b32 s86, s86, 1\n\t"
        "s_and_b32 s87, s13, 1\n\t"
        "s_or_b32 s86, s86, s87\n\t"
        "s_cmp_lt_u32 s13, 18\n\t"
        "s_cselect_b32 s86, s86, 64\n\t"
        "s_mul_i32 s92, s86, 589824\n\t"
        "s_lshr_b32 s86, 0x9070605, s88\n\t"
        "s_and_b32 s86, s86, 31\n\t"
        "s_lshl_b32 s96, s86, 12\n\t"
        "s_and_b32 s86, s100, 1\n\t"
        "s_lshl_b32 s86, s86, 8\n\t"
        "s_add_u32 s92, s92, s86\n\t"
        "s_cmp_eq_u32 s100, 2\n\t"
        "s_cselect_b64 vcc, -1, 0\n\t"
        "v_cndmask_b32 v146, v144, v145, vcc\n\t"
        "v_cndmask_b32 v150, v148, v149, vcc\n\t"
        "v_add_u32 v150, s86, v150\n\t"
        "buffer_load_dwordx4 v[0:3], v146, s[20:23], s92 offen\n\t"
        "s_add_u32 s58, s92, 18432\n\t"
        "buffer_load_dwordx4 v[4:7], v146, s[20:23], s58 offen\n\t"
        "s_add_u32 s58, s92, 36864\n\t"
        "buffer_load_dwordx4 v[8:11], v146, s[20:23], s58 offen\n\t"
        "s_add_u32 s58, s92, 55296\n\t"
        "buffer_load_dwordx4 v[12:15], v146, s[20:23], s58 offen\n\t"
        "s_add_u32 s58, s92, 73728\n\t"
        "buffer_load_dwordx4 v[16:19], v146, s[20:23], s58 offen\n\t"
        "s_add_u32 s58, s92, 92160\n\t"
        "buffer_load_dwordx4 v[20:23], v146, s[20:23], s58 offen\n\t"
        "s_add_u32 s58, s92, 110592\n\t"
        "buffer_load_dwordx4 v[24:27], v146, s[20:23], s58 offen\n\t"
        "s_add_u32 s58, s92, 129024\n\t"
        "buffer_load_dwordx4 v[28:31], v146, s[20:23], s58 offen\n\t"
        "s_waitcnt vmcnt(15) lgkmcnt(0)\n\t"
        "v_fma_mix_f32 v96, v96, v128, v132 op_sel_hi:[1,0,0]\n\t"
        "s_waitcnt vmcnt(14)\n\t"
        "v_fma_mix_f32 v100, v100, v128, v132 op_sel_hi:[1,0,0]\n\t"
        "s_waitcnt vmcnt(13)\n\t"
        "v_fma_mix_f32 v104, v104, v128, v132 op_sel_hi:[1,0,0]\n\t"
        "s_waitcnt vmcnt(12)\n\t"
        "v_fma_mix_f32 v108, v108, v128, v132 op_sel_hi:[1,0,0]\n\t"
        "s_waitcnt vmcnt(11)\n\t"
        "v_fma_mix_f32 v112, v112, v128, v132 op_sel_hi:[1,0,0]\n\t"
        "s_waitcnt vmcnt(10)\n\t"
        "v_fma_mix_f32 v116, v116, v128, v132 op_sel_hi:[1,0,0]\n\t"
        "s_waitcnt vmcnt(9)\n\t"
        "v_fma_mix_f32 v120, v120, v128, v132 op_sel_hi:[1,0,0]\n\t"
        "s_waitcnt vmcnt(8)\n\t"
        "v_fma_mix_f32 v124, v124, v128, v132 op_sel_hi:[1,0,0]\n\t"
        "v_cvt_pk_bf16_f32 v136, v96, v100\n\t"
        "v_cvt_pk_bf16_f32 v137, v104, v108\n\t"
        "v_cvt_pk_bf16_f32 v138, v112, v116\n\t"
        "v_cvt_pk_bf16_f32 v139, v120, v124\n\t"
        "ds_write_b128 v147, v[136:139] offset:0\n\t"
        "s_waitcnt lgkmcnt(0)\n\t"
        "s_barrier\n\t"
        "v_fma_mix_f32 v97, v97, v129, v133 op_sel_hi:[1,0,0]\n\t"
        "v_fma_mix_f32 v101, v101, v129, v133 op_sel_hi:[1,0,0]\n\t"
        "v_fma_mix_f32 v105, v105, v129, v133 op_sel_hi:[1,0,0]\n\t"
        "v_fma_mix_f32 v109, v109, v129, v133 op_sel_hi:[1,0,0]\n\t"
        "v_fma_mix_f32 v113, v113, v129, v133 op_sel_hi:[1,0,0]\n\t"
        "v_fma_mix_f32 v117, v117, v129, v133 op_sel_hi:[1,0,0]\n\t"
        "v_fma_mix_f32 v121, v121, v129, v133 op_sel_hi:[1,0,0]\n\t"
        "v_fma_mix_f32 v125, v125, v129, v133 op_sel_hi:[1,0,0]\n\t"
        "v_cvt_pk_bf16_f32 v140, v97, v101\n\t"
        "v_cvt_pk_bf16_f32 v141, v105, v109\n\t"
        "v_cvt_pk_bf16_f32 v142, v113, v117\n\t"
        "v_cvt_pk_bf16_f32 v143, v121, v125\n\t"
        "v_xor_b32 v164, s89, v147\n\t"
        "ds_write_b128 v164, v[140:143] offset:1024\n\t"
        "v_fma_mix_f32 v98, v98, v130, v134 op_sel_hi:[1,0,0]\n\t"
        "v_fma_mix_f32 v102, v102, v130, v134 op_sel_hi:[1,0,0]\n\t"
        "v_fma_mix_f32 v106, v106, v130, v134 op_sel_hi:[1,0,0]\n\t"
        "v_fma_mix_f32 v110, v110, v130, v134 op_sel_hi:[1,0,0]\n\t"
        "v_fma_mix_f32 v114, v114, v130, v134 op_sel_hi:[1,0,0]\n\t"
        "v_fma_mix_f32 v118, v118, v130, v134 op_sel_hi:[1,0,0]\n\t"
        "v_fma_mix_f32 v122, v122, v130, v134 op_sel_hi:[1,0,0]\n\t"
        "v_fma_mix_f32 v126, v126, v130, v134 op_sel_hi:[1,0,0]\n\t"
        "v_cvt_pk_bf16_f32 v136, v98, v102\n\t"
        "v_cvt_pk_bf16_f32 v137, v106, v110\n\t"
        "v_cvt_pk_bf16_f32 v138, v114, v118\n\t"
        "v_cvt_pk_bf16_f32 v139, v122, v126\n\t"
        "v_xor_b32 v164, s90, v147\n\t"
        "ds_write_b128 v164, v[136:139] offset:2048\n\t"
        "v_fma_mix_f32 v99, v99, v131, v135 op_sel_hi:[1,0,0]\n\t"
        "v_fma_mix_f32 v103, v103, v131, v135 op_sel_hi:[1,0,0]\n\t"
        "v_fma_mix_f32 v107, v107, v131, v135 op_sel_hi:[1,0,0]\n\t"
        "v_fma_mix_f32 v111, v111, v131, v135 op_sel_hi:[1,0,0]\n\t"
        "v_fma_mix_f32 v115, v115, v131, v135 op_sel_hi:[1,0,0]\n\t"
        "v_fma_mix_f32 v119, v119, v131, v135 op_sel_hi:[1,0,0]\n\t"
        "v_fma_mix_f32 v123, v123, v131, v135 op_sel_hi:[1,0,0]\n\t"
        "v_fma_mix_f32 v127, v127, v131, v135 op_sel_hi:[1,0,0]\n\t"
        "v_cvt_pk_bf16_f32 v140, v99, v103\n\t"
        "v_cvt_pk_bf16_f32 v141, v107, v111\n\t"
        "v_cvt_pk_bf16_f32 v142, v115, v119\n\t"
        "v_cvt_pk_bf16_f32 v143, v123, v127\n\t"
        "v_xor_b32 v164, s91, v147\n\t"
        "ds_write_b128 v164, v[140:143] offset:3072\n\t"
        "s_waitcnt lgkmcnt(0)\n\t"
        "s_barrier\n\t"
        "ds_read_b128 v[128:131], v150 offset:45056\n\t"
        "ds_read_b128 v[132:135], v150 offset:45824\n\t"
        "s_cmp_eq_u32 s100, 2\n\t"
        "s_cselect_b32 s89, 32, 0\n\t"
        "s_cselect_b32 s90, 64, 0\n\t"
        "s_cselect_b32 s91, 0x60, 0\n\t"
        "v_add_u32 v147, s96, v166\n\t"
        "s_lshr_b32 s86, 0x2c2b2a11, s88\n\t"
        "s_bfe_u32 s101, s86, 0x20005\n\t"
        "s_and_b32 s13, s86, 31\n\t"
        "s_lshr_b32 s86, s13, 1\n\t"
        "s_lshl_b32 s86, s86, 2\n\t"
        "s_lshr_b64 s[86:87], s[84:85], s86\n\t"
        "s_and_b32 s86, s86, 15\n\t"
        "s_lshl_b32 s86, s86, 1\n\t"
        "s_and_b32 s87, s13, 1\n\t"
        "s_or_b32 s86, s86, s87\n\t"
        "s_cmp_lt_u32 s13, 18\n\t"
        "s_cselect_b32 s86, s86, 64\n\t"
        "s_mul_i32 s93, s86, 589824\n\t"
        "s_lshr_b32 s86, 0x3020100, s88\n\t"
        "s_and_b32 s86, s86, 31\n\t"
        "s_lshl_b32 s97, s86, 12\n\t"
        "s_and_b32 s86, s101, 1\n\t"
        "s_lshl_b32 s86, s86, 8\n\t"
        "s_add_u32 s93, s93, s86\n\t"
        "s_cmp_eq_u32 s101, 2\n\t"
        "s_cselect_b64 vcc, -1, 0\n\t"
        "v_cndmask_b32 v146, v144, v145, vcc\n\t"
        "v_cndmask_b32 v151, v148, v149, vcc\n\t"
        "v_add_u32 v151, s86, v151\n\t"
        "buffer_load_dwordx4 v[32:35], v146, s[20:23], s93 offen\n\t"
        "s_add_u32 s58, s93, 18432\n\t"
        "buffer_load_dwordx4 v[36:39], v146, s[20:23], s58 offen\n\t"
        "s_add_u32 s58, s93, 36864\n\t"
        "buffer_load_dwordx4 v[40:43], v146, s[20:23], s58 offen\n\t"
        "s_add_u32 s58, s93, 55296\n\t"
        "buffer_load_dwordx4 v[44:47], v146, s[20:23], s58 offen\n\t"
        "s_add_u32 s58, s93, 73728\n\t"
        "buffer_load_dwordx4 v[48:51], v146, s[20:23], s58 offen\n\t"
        "s_add_u32 s58, s93, 92160\n\t"
        "buffer_load_dwordx4 v[52:55], v146, s[20:23], s58 offen\n\t"
        "s_add_u32 s58, s93, 110592\n\t"
        "buffer_load_dwordx4 v[56:59], v146, s[20:23], s58 offen\n\t"
        "s_add_u32 s58, s93, 129024\n\t"
        "buffer_load_dwordx4 v[60:63], v146, s[20:23], s58 offen\n\t"
        "s_waitcnt vmcnt(15) lgkmcnt(0)\n\t"
        "v_fma_mix_f32 v0, v0, v128, v132 op_sel_hi:[1,0,0]\n\t"
        "s_waitcnt vmcnt(14)\n\t"
        "v_fma_mix_f32 v4, v4, v128, v132 op_sel_hi:[1,0,0]\n\t"
        "s_waitcnt vmcnt(13)\n\t"
        "v_fma_mix_f32 v8, v8, v128, v132 op_sel_hi:[1,0,0]\n\t"
        "s_waitcnt vmcnt(12)\n\t"
        "v_fma_mix_f32 v12, v12, v128, v132 op_sel_hi:[1,0,0]\n\t"
        "s_waitcnt vmcnt(11)\n\t"
        "v_fma_mix_f32 v16, v16, v128, v132 op_sel_hi:[1,0,0]\n\t"
        "s_waitcnt vmcnt(10)\n\t"
        "v_fma_mix_f32 v20, v20, v128, v132 op_sel_hi:[1,0,0]\n\t"
        "s_waitcnt vmcnt(9)\n\t"
        "v_fma_mix_f32 v24, v24, v128, v132 op_sel_hi:[1,0,0]\n\t"
        "s_waitcnt vmcnt(8)\n\t"
        "v_fma_mix_f32 v28, v28, v128, v132 op_sel_hi:[1,0,0]\n\t"
        "v_cvt_pk_bf16_f32 v136, v0, v4\n\t"
        "v_cvt_pk_bf16_f32 v137, v8, v12\n\t"
        "v_cvt_pk_bf16_f32 v138, v16, v20\n\t"
        "v_cvt_pk_bf16_f32 v139, v24, v28\n\t"
        "ds_write_b128 v147, v[136:139] offset:0\n\t"
        "s_waitcnt lgkmcnt(0)\n\t"
        "s_barrier\n\t"
        "v_fma_mix_f32 v1, v1, v129, v133 op_sel_hi:[1,0,0]\n\t"
        "v_fma_mix_f32 v5, v5, v129, v133 op_sel_hi:[1,0,0]\n\t"
        "v_fma_mix_f32 v9, v9, v129, v133 op_sel_hi:[1,0,0]\n\t"
        "v_fma_mix_f32 v13, v13, v129, v133 op_sel_hi:[1,0,0]\n\t"
        "v_fma_mix_f32 v17, v17, v129, v133 op_sel_hi:[1,0,0]\n\t"
        "v_fma_mix_f32 v21, v21, v129, v133 op_sel_hi:[1,0,0]\n\t"
        "v_fma_mix_f32 v25, v25, v129, v133 op_sel_hi:[1,0,0]\n\t"
        "v_fma_mix_f32 v29, v29, v129, v133 op_sel_hi:[1,0,0]\n\t"
        "v_cvt_pk_bf16_f32 v140, v1, v5\n\t"
        "v_cvt_pk_bf16_f32 v141, v9, v13\n\t"
        "v_cvt_pk_bf16_f32 v142, v17, v21\n\t"
        "v_cvt_pk_bf16_f32 v143, v25, v29\n\t"
        "v_xor_b32 v164, s89, v147\n\t"
        "ds_write_b128 v164, v[140:143] offset:1024\n\t"
        "v_fma_mix_f32 v2, v2, v130, v134 op_sel_hi:[1,0,0]\n\t"
        "v_fma_mix_f32 v6, v6, v130, v134 op_sel_hi:[1,0,0]\n\t"
        "v_fma_mix_f32 v10, v10, v130, v134 op_sel_hi:[1,0,0]\n\t"
        "v_fma_mix_f32 v14, v14, v130, v134 op_sel_hi:[1,0,0]\n\t"
        "v_fma_mix_f32 v18, v18, v130, v134 op_sel_hi:[1,0,0]\n\t"
        "v_fma_mix_f32 v22, v22, v130, v134 op_sel_hi:[1,0,0]\n\t"
        "v_fma_mix_f32 v26, v26, v130, v134 op_sel_hi:[1,0,0]\n\t"
        "v_fma_mix_f32 v30, v30, v130, v134 op_sel_hi:[1,0,0]\n\t"
        "v_cvt_pk_bf16_f32 v136, v2, v6\n\t"
        "v_cvt_pk_bf16_f32 v137, v10, v14\n\t"
        "v_cvt_pk_bf16_f32 v138, v18, v22\n\t"
        "v_cvt_pk_bf16_f32 v139, v26, v30\n\t"
        "v_xor_b32 v164, s90, v147\n\t"
        "ds_write_b128 v164, v[136:139] offset:2048\n\t"
        "v_fma_mix_f32 v3, v3, v131, v135 op_sel_hi:[1,0,0]\n\t"
        "v_fma_mix_f32 v7, v7, v131, v135 op_sel_hi:[1,0,0]\n\t"
        "v_fma_mix_f32 v11, v11, v131, v135 op_sel_hi:[1,0,0]\n\t"
        "v_fma_mix_f32 v15, v15, v131, v135 op_sel_hi:[1,0,0]\n\t"
        "v_fma_mix_f32 v19, v19, v131, v135 op_sel_hi:[1,0,0]\n\t"
        "v_fma_mix_f32 v23, v23, v131, v135 op_sel_hi:[1,0,0]\n\t"
        "v_fma_mix_f32 v27, v27, v131, v135 op_sel_hi:[1,0,0]\n\t"
        "v_fma_mix_f32 v31, v31, v131, v135 op_sel_hi:[1,0,0]\n\t"
        "v_cvt_pk_bf16_f32 v140, v3, v7\n\t"
        "v_cvt_pk_bf16_f32 v141, v11, v15\n\t"
        "v_cvt_pk_bf16_f32 v142, v19, v23\n\t"
        "v_cvt_pk_bf16_f32 v143, v27, v31\n\t"
        "v_xor_b32 v164, s91, v147\n\t"
        "ds_write_b128 v164, v[140:143] offset:3072\n\t"
        "s_waitcnt lgkmcnt(0)\n\t"
        "s_barrier\n\t"
        "ds_read_b128 v[128:131], v151 offset:45056\n\t"
        "ds_read_b128 v[132:135], v151 offset:45824\n\t"
        "s_cmp_eq_u32 s101, 2\n\t"
        "s_cselect_b32 s89, 32, 0\n\t"
        "s_cselect_b32 s90, 64, 0\n\t"
        "s_cselect_b32 s91, 0x60, 0\n\t"
        "v_add_u32 v147, s97, v166\n\t"
        "s_lshr_b32 s86, 0x302f2e2d, s88\n\t"
        "s_bfe_u32 s18, s86, 0x20005\n\t"
        "s_and_b32 s13, s86, 31\n\t"
        "s_lshr_b32 s86, s13, 1\n\t"
        "s_lshl_b32 s86, s86, 2\n\t"
        "s_lshr_b64 s[86:87], s[84:85], s86\n\t"
        "s_and_b32 s86, s86, 15\n\t"
        "s_lshl_b32 s86, s86, 1\n\t"
        "s_and_b32 s87, s13, 1\n\t"
        "s_or_b32 s86, s86, s87\n\t"
        "s_cmp_lt_u32 s13, 18\n\t"
        "s_cselect_b32 s86, s86, 64\n\t"
        "s_mul_i32 s94, s86, 589824\n\t"
        "s_lshr_b32 s86, 0x6050804, s88\n\t"
        "s_and_b32 s86, s86, 31\n\t"
        "s_lshl_b32 s98, s86, 12\n\t"
        "s_and_b32 s86, s18, 1\n\t"
        "s_lshl_b32 s86, s86, 8\n\t"
        "s_add_u32 s94, s94, s86\n\t"
        "s_cmp_eq_u32 s18, 2\n\t"
        "s_cselect_b64 vcc, -1, 0\n\t"
        "v_cndmask_b32 v146, v144, v145, vcc\n\t"
        "v_cndmask_b32 v152, v148, v149, vcc\n\t"
        "v_add_u32 v152, s86, v152\n\t"
        "buffer_load_dwordx4 v[64:67], v146, s[20:23], s94 offen\n\t"
        "s_add_u32 s58, s94, 18432\n\t"
        "buffer_load_dwordx4 v[68:71], v146, s[20:23], s58 offen\n\t"
        "s_add_u32 s58, s94, 36864\n\t"
        "buffer_load_dwordx4 v[72:75], v146, s[20:23], s58 offen\n\t"
        "s_add_u32 s58, s94, 55296\n\t"
        "buffer_load_dwordx4 v[76:79], v146, s[20:23], s58 offen\n\t"
        "s_add_u32 s58, s94, 73728\n\t"
        "buffer_load_dwordx4 v[80:83], v146, s[20:23], s58 offen\n\t"
        "s_add_u32 s58, s94, 92160\n\t"
        "buffer_load_dwordx4 v[84:87], v146, s[20:23], s58 offen\n\t"
        "s_add_u32 s58, s94, 110592\n\t"
        "buffer_load_dwordx4 v[88:91], v146, s[20:23], s58 offen\n\t"
        "s_add_u32 s58, s94, 129024\n\t"
        "buffer_load_dwordx4 v[92:95], v146, s[20:23], s58 offen\n\t"
        "s_waitcnt vmcnt(15) lgkmcnt(0)\n\t"
        "v_fma_mix_f32 v32, v32, v128, v132 op_sel_hi:[1,0,0]\n\t"
        "s_waitcnt vmcnt(14)\n\t"
        "v_fma_mix_f32 v36, v36, v128, v132 op_sel_hi:[1,0,0]\n\t"
        "s_waitcnt vmcnt(13)\n\t"
        "v_fma_mix_f32 v40, v40, v128, v132 op_sel_hi:[1,0,0]\n\t"
        "s_waitcnt vmcnt(12)\n\t"
        "v_fma_mix_f32 v44, v44, v128, v132 op_sel_hi:[1,0,0]\n\t"
        "s_waitcnt vmcnt(11)\n\t"
        "v_fma_mix_f32 v48, v48, v128, v132 op_sel_hi:[1,0,0]\n\t"
        "s_waitcnt vmcnt(10)\n\t"
        "v_fma_mix_f32 v52, v52, v128, v132 op_sel_hi:[1,0,0]\n\t"
        "s_waitcnt vmcnt(9)\n\t"
        "v_fma_mix_f32 v56, v56, v128, v132 op_sel_hi:[1,0,0]\n\t"
        "s_waitcnt vmcnt(8)\n\t"
        "v_fma_mix_f32 v60, v60, v128, v132 op_sel_hi:[1,0,0]\n\t"
        "v_cvt_pk_bf16_f32 v136, v32, v36\n\t"
        "v_cvt_pk_bf16_f32 v137, v40, v44\n\t"
        "v_cvt_pk_bf16_f32 v138, v48, v52\n\t"
        "v_cvt_pk_bf16_f32 v139, v56, v60\n\t"
        "ds_write_b128 v147, v[136:139] offset:0\n\t"
        "s_waitcnt lgkmcnt(0)\n\t"
        "s_barrier\n\t"
        "v_fma_mix_f32 v33, v33, v129, v133 op_sel_hi:[1,0,0]\n\t"
        "v_fma_mix_f32 v37, v37, v129, v133 op_sel_hi:[1,0,0]\n\t"
        "v_fma_mix_f32 v41, v41, v129, v133 op_sel_hi:[1,0,0]\n\t"
        "v_fma_mix_f32 v45, v45, v129, v133 op_sel_hi:[1,0,0]\n\t"
        "v_fma_mix_f32 v49, v49, v129, v133 op_sel_hi:[1,0,0]\n\t"
        "v_fma_mix_f32 v53, v53, v129, v133 op_sel_hi:[1,0,0]\n\t"
        "v_fma_mix_f32 v57, v57, v129, v133 op_sel_hi:[1,0,0]\n\t"
        "v_fma_mix_f32 v61, v61, v129, v133 op_sel_hi:[1,0,0]\n\t"
        "v_cvt_pk_bf16_f32 v140, v33, v37\n\t"
        "v_cvt_pk_bf16_f32 v141, v41, v45\n\t"
        "v_cvt_pk_bf16_f32 v142, v49, v53\n\t"
        "v_cvt_pk_bf16_f32 v143, v57, v61\n\t"
        "v_xor_b32 v164, s89, v147\n\t"
        "ds_write_b128 v164, v[140:143] offset:1024\n\t"
        "v_fma_mix_f32 v34, v34, v130, v134 op_sel_hi:[1,0,0]\n\t"
        "v_fma_mix_f32 v38, v38, v130, v134 op_sel_hi:[1,0,0]\n\t"
        "v_fma_mix_f32 v42, v42, v130, v134 op_sel_hi:[1,0,0]\n\t"
        "v_fma_mix_f32 v46, v46, v130, v134 op_sel_hi:[1,0,0]\n\t"
        "v_fma_mix_f32 v50, v50, v130, v134 op_sel_hi:[1,0,0]\n\t"
        "v_fma_mix_f32 v54, v54, v130, v134 op_sel_hi:[1,0,0]\n\t"
        "v_fma_mix_f32 v58, v58, v130, v134 op_sel_hi:[1,0,0]\n\t"
        "v_fma_mix_f32 v62, v62, v130, v134 op_sel_hi:[1,0,0]\n\t"
        "v_cvt_pk_bf16_f32 v136, v34, v38\n\t"
        "v_cvt_pk_bf16_f32 v137, v42, v46\n\t"
        "v_cvt_pk_bf16_f32 v138, v50, v54\n\t"
        "v_cvt_pk_bf16_f32 v139, v58, v62\n\t"
        "v_xor_b32 v164, s90, v147\n\t"
        "ds_write_b128 v164, v[136:139] offset:2048\n\t"
        "v_fma_mix_f32 v35, v35, v131, v135 op_sel_hi:[1,0,0]\n\t"
        "v_fma_mix_f32 v39, v39, v131, v135 op_sel_hi:[1,0,0]\n\t"
        "v_fma_mix_f32 v43, v43, v131, v135 op_sel_hi:[1,0,0]\n\t"
        "v_fma_mix_f32 v47, v47, v131, v135 op_sel_hi:[1,0,0]\n\t"
        "v_fma_mix_f32 v51, v51, v131, v135 op_sel_hi:[1,0,0]\n\t"
        "v_fma_mix_f32 v55, v55, v131, v135 op_sel_hi:[1,0,0]\n\t"
        "v_fma_mix_f32 v59, v59, v131, v135 op_sel_hi:[1,0,0]\n\t"
        "v_fma_mix_f32 v63, v63, v131, v135 op_sel_hi:[1,0,0]\n\t"
        "v_cvt_pk_bf16_f32 v140, v35, v39\n\t"
        "v_cvt_pk_bf16_f32 v141, v43, v47\n\t"
        "v_cvt_pk_bf16_f32 v142, v51, v55\n\t"
        "v_cvt_pk_bf16_f32 v143, v59, v63\n\t"
        "v_xor_b32 v164, s91, v147\n\t"
        "ds_write_b128 v164, v[140:143] offset:3072\n\t"
        "s_waitcnt lgkmcnt(0)\n\t"
        "s_barrier\n\t"
        "ds_read_b128 v[128:131], v152 offset:45056\n\t"
        "ds_read_b128 v[132:135], v152 offset:45824\n\t"
        "s_cmp_eq_u32 s18, 2\n\t"
        "s_cselect_b32 s89, 32, 0\n\t"
        "s_cselect_b32 s90, 64, 0\n\t"
        "s_cselect_b32 s91, 0x60, 0\n\t"
        "v_add_u32 v147, s98, v166\n\t"
        "s_lshr_b32 s86, 0x1f1f1f31, s88\n\t"
        "s_bfe_u32 s19, s86, 0x20005\n\t"
        "s_and_b32 s13, s86, 31\n\t"
        "s_lshr_b32 s86, s13, 1\n\t"
        "s_lshl_b32 s86, s86, 2\n\t"
        "s_lshr_b64 s[86:87], s[84:85], s86\n\t"
        "s_and_b32 s86, s86, 15\n\t"
        "s_lshl_b32 s86, s86, 1\n\t"
        "s_and_b32 s87, s13, 1\n\t"
        "s_or_b32 s86, s86, s87\n\t"
        "s_cmp_lt_u32 s13, 18\n\t"
        "s_cselect_b32 s86, s86, 64\n\t"
        "s_mul_i32 s95, s86, 589824\n\t"
        "s_lshr_b32 s86, 0xa0a0a00, s88\n\t"
        "s_and_b32 s86, s86, 31\n\t"
        "s_lshl_b32 s99, s86, 12\n\t"
        "s_and_b32 s86, s19, 1\n\t"
        "s_lshl_b32 s86, s86, 8\n\t"
        "s_add_u32 s95, s95, s86\n\t"
        "s_cmp_eq_u32 s19, 2\n\t"
        "s_cselect_b64 vcc, -1, 0\n\t"
        "v_cndmask_b32 v146, v144, v145, vcc\n\t"
        "v_cndmask_b32 v153, v148, v149, vcc\n\t"
        "v_add_u32 v153, s86, v153\n\t"
        "buffer_load_dwordx4 v[96:99], v146, s[20:23], s95 offen\n\t"
        "s_add_u32 s58, s95, 18432\n\t"
        "buffer_load_dwordx4 v[100:103], v146, s[20:23], s58 offen\n\t"
        "s_add_u32 s58, s95, 36864\n\t"
        "buffer_load_dwordx4 v[104:107], v146, s[20:23], s58 offen\n\t"
        "s_add_u32 s58, s95, 55296\n\t"
        "buffer_load_dwordx4 v[108:111], v146, s[20:23], s58 offen\n\t"
        "s_add_u32 s58, s95, 73728\n\t"
        "buffer_load_dwordx4 v[112:115], v146, s[20:23], s58 offen\n\t"
        "s_add_u32 s58, s95, 92160\n\t"
        "buffer_load_dwordx4 v[116:119], v146, s[20:23], s58 offen\n\t"
        "s_add_u32 s58, s95, 110592\n\t"
        "buffer_load_dwordx4 v[120:123], v146, s[20:23], s58 offen\n\t"
        "s_add_u32 s58, s95, 129024\n\t"
        "buffer_load_dwordx4 v[124:127], v146, s[20:23], s58 offen\n\t"
        "s_waitcnt vmcnt(15) lgkmcnt(0)\n\t"
        "v_fma_mix_f32 v64, v64, v128, v132 op_sel_hi:[1,0,0]\n\t"
        "s_waitcnt vmcnt(14)\n\t"
        "v_fma_mix_f32 v68, v68, v128, v132 op_sel_hi:[1,0,0]\n\t"
        "s_waitcnt vmcnt(13)\n\t"
        "v_fma_mix_f32 v72, v72, v128, v132 op_sel_hi:[1,0,0]\n\t"
        "s_waitcnt vmcnt(12)\n\t"
        "v_fma_mix_f32 v76, v76, v128, v132 op_sel_hi:[1,0,0]\n\t"
        "s_waitcnt vmcnt(11)\n\t"
        "v_fma_mix_f32 v80, v80, v128, v132 op_sel_hi:[1,0,0]\n\t"
        "s_waitcnt vmcnt(10)\n\t"
        "v_fma_mix_f32 v84, v84, v128, v132 op_sel_hi:[1,0,0]\n\t"
        "s_waitcnt vmcnt(9)\n\t"
        "v_fma_mix_f32 v88, v88, v128, v132 op_sel_hi:[1,0,0]\n\t"
        "s_waitcnt vmcnt(8)\n\t"
        "v_fma_mix_f32 v92, v92, v128, v132 op_sel_hi:[1,0,0]\n\t"
        "v_cvt_pk_bf16_f32 v136, v64, v68\n\t"
        "v_cvt_pk_bf16_f32 v137, v72, v76\n\t"
        "v_cvt_pk_bf16_f32 v138, v80, v84\n\t"
        "v_cvt_pk_bf16_f32 v139, v88, v92\n\t"
        "ds_write_b128 v147, v[136:139] offset:0\n\t"
        "s_waitcnt lgkmcnt(0)\n\t"
        "s_barrier\n\t"
        "v_fma_mix_f32 v65, v65, v129, v133 op_sel_hi:[1,0,0]\n\t"
        "v_fma_mix_f32 v69, v69, v129, v133 op_sel_hi:[1,0,0]\n\t"
        "v_fma_mix_f32 v73, v73, v129, v133 op_sel_hi:[1,0,0]\n\t"
        "v_fma_mix_f32 v77, v77, v129, v133 op_sel_hi:[1,0,0]\n\t"
        "v_fma_mix_f32 v81, v81, v129, v133 op_sel_hi:[1,0,0]\n\t"
        "v_fma_mix_f32 v85, v85, v129, v133 op_sel_hi:[1,0,0]\n\t"
        "v_fma_mix_f32 v89, v89, v129, v133 op_sel_hi:[1,0,0]\n\t"
        "v_fma_mix_f32 v93, v93, v129, v133 op_sel_hi:[1,0,0]\n\t"
        "v_cvt_pk_bf16_f32 v140, v65, v69\n\t"
        "v_cvt_pk_bf16_f32 v141, v73, v77\n\t"
        "v_cvt_pk_bf16_f32 v142, v81, v85\n\t"
        "v_cvt_pk_bf16_f32 v143, v89, v93\n\t"
        "v_xor_b32 v164, s89, v147\n\t"
        "ds_write_b128 v164, v[140:143] offset:1024\n\t"
        "v_fma_mix_f32 v66, v66, v130, v134 op_sel_hi:[1,0,0]\n\t"
        "v_fma_mix_f32 v70, v70, v130, v134 op_sel_hi:[1,0,0]\n\t"
        "v_fma_mix_f32 v74, v74, v130, v134 op_sel_hi:[1,0,0]\n\t"
        "v_fma_mix_f32 v78, v78, v130, v134 op_sel_hi:[1,0,0]\n\t"
        "v_fma_mix_f32 v82, v82, v130, v134 op_sel_hi:[1,0,0]\n\t"
        "v_fma_mix_f32 v86, v86, v130, v134 op_sel_hi:[1,0,0]\n\t"
        "v_fma_mix_f32 v90, v90, v130, v134 op_sel_hi:[1,0,0]\n\t"
        "v_fma_mix_f32 v94, v94, v130, v134 op_sel_hi:[1,0,0]\n\t"
        "v_cvt_pk_bf16_f32 v136, v66, v70\n\t"
        "v_cvt_pk_bf16_f32 v137, v74, v78\n\t"
        "v_cvt_pk_bf16_f32 v138, v82, v86\n\t"
        "v_cvt_pk_bf16_f32 v139, v90, v94\n\t"
        "v_xor_b32 v164, s90, v147\n\t"
        "ds_write_b128 v164, v[136:139] offset:2048\n\t"
        "v_fma_mix_f32 v67, v67, v131, v135 op_sel_hi:[1,0,0]\n\t"
        "v_fma_mix_f32 v71, v71, v131, v135 op_sel_hi:[1,0,0]\n\t"
        "v_fma_mix_f32 v75, v75, v131, v135 op_sel_hi:[1,0,0]\n\t"
        "v_fma_mix_f32 v79, v79, v131, v135 op_sel_hi:[1,0,0]\n\t"
        "v_fma_mix_f32 v83, v83, v131, v135 op_sel_hi:[1,0,0]\n\t"
        "v_fma_mix_f32 v87, v87, v131, v135 op_sel_hi:[1,0,0]\n\t"
        "v_fma_mix_f32 v91, v91, v131, v135 op_sel_hi:[1,0,0]\n\t"
        "v_fma_mix_f32 v95, v95, v131, v135 op_sel_hi:[1,0,0]\n\t"
        "v_cvt_pk_bf16_f32 v140, v67, v71\n\t"
        "v_cvt_pk_bf16_f32 v141, v75, v79\n\t"
        "v_cvt_pk_bf16_f32 v142, v83, v87\n\t"
        "v_cvt_pk_bf16_f32 v143, v91, v95\n\t"
        "v_xor_b32 v164, s91, v147\n\t"
        "ds_write_b128 v164, v[140:143] offset:3072\n\t"
        "s_waitcnt lgkmcnt(0)\n\t"
        "s_barrier\n\t"
        "ds_read_b128 v[128:131], v153 offset:45056\n\t"
        "ds_read_b128 v[132:135], v153 offset:45824\n\t"
        "s_cmp_eq_u32 s19, 2\n\t"
        "s_cselect_b32 s89, 32, 0\n\t"
        "s_cselect_b32 s90, 64, 0\n\t"
        "s_cselect_b32 s91, 0x60, 0\n\t"
        "v_add_u32 v147, s99, v166\n\t"
        "s_waitcnt vmcnt(7) lgkmcnt(0)\n\t"
        "v_fma_mix_f32 v96, v96, v128, v132 op_sel_hi:[1,0,0]\n\t"
        "s_waitcnt vmcnt(6)\n\t"
        "v_fma_mix_f32 v100, v100, v128, v132 op_sel_hi:[1,0,0]\n\t"
        "s_waitcnt vmcnt(5)\n\t"
        "v_fma_mix_f32 v104, v104, v128, v132 op_sel_hi:[1,0,0]\n\t"
        "s_waitcnt vmcnt(4)\n\t"
        "v_fma_mix_f32 v108, v108, v128, v132 op_sel_hi:[1,0,0]\n\t"
        "s_waitcnt vmcnt(3)\n\t"
        "v_fma_mix_f32 v112, v112, v128, v132 op_sel_hi:[1,0,0]\n\t"
        "s_waitcnt vmcnt(2)\n\t"
        "v_fma_mix_f32 v116, v116, v128, v132 op_sel_hi:[1,0,0]\n\t"
        "s_waitcnt vmcnt(1)\n\t"
        "v_fma_mix_f32 v120, v120, v128, v132 op_sel_hi:[1,0,0]\n\t"
        "s_waitcnt vmcnt(0)\n\t"
        "v_fma_mix_f32 v124, v124, v128, v132 op_sel_hi:[1,0,0]\n\t"
        "v_cvt_pk_bf16_f32 v136, v96, v100\n\t"
        "v_cvt_pk_bf16_f32 v137, v104, v108\n\t"
        "v_cvt_pk_bf16_f32 v138, v112, v116\n\t"
        "v_cvt_pk_bf16_f32 v139, v120, v124\n\t"
        "ds_write_b128 v147, v[136:139] offset:0\n\t"
        "s_waitcnt lgkmcnt(0)\n\t"
        "s_barrier\n\t"
        "v_fma_mix_f32 v97, v97, v129, v133 op_sel_hi:[1,0,0]\n\t"
        "v_fma_mix_f32 v101, v101, v129, v133 op_sel_hi:[1,0,0]\n\t"
        "v_fma_mix_f32 v105, v105, v129, v133 op_sel_hi:[1,0,0]\n\t"
        "v_fma_mix_f32 v109, v109, v129, v133 op_sel_hi:[1,0,0]\n\t"
        "v_fma_mix_f32 v113, v113, v129, v133 op_sel_hi:[1,0,0]\n\t"
        "v_fma_mix_f32 v117, v117, v129, v133 op_sel_hi:[1,0,0]\n\t"
        "v_fma_mix_f32 v121, v121, v129, v133 op_sel_hi:[1,0,0]\n\t"
        "v_fma_mix_f32 v125, v125, v129, v133 op_sel_hi:[1,0,0]\n\t"
        "v_cvt_pk_bf16_f32 v140, v97, v101\n\t"
        "v_cvt_pk_bf16_f32 v141, v105, v109\n\t"
        "v_cvt_pk_bf16_f32 v142, v113, v117\n\t"
        "v_cvt_pk_bf16_f32 v143, v121, v125\n\t"
        "v_xor_b32 v164, s89, v147\n\t"
        "ds_write_b128 v164, v[140:143] offset:1024\n\t"
        "v_fma_mix_f32 v98, v98, v130, v134 op_sel_hi:[1,0,0]\n\t"
        "v_fma_mix_f32 v102, v102, v130, v134 op_sel_hi:[1,0,0]\n\t"
        "v_fma_mix_f32 v106, v106, v130, v134 op_sel_hi:[1,0,0]\n\t"
        "v_fma_mix_f32 v110, v110, v130, v134 op_sel_hi:[1,0,0]\n\t"
        "v_fma_mix_f32 v114, v114, v130, v134 op_sel_hi:[1,0,0]\n\t"
        "v_fma_mix_f32 v118, v118, v130, v134 op_sel_hi:[1,0,0]\n\t"
        "v_fma_mix_f32 v122, v122, v130, v134 op_sel_hi:[1,0,0]\n\t"
        "v_fma_mix_f32 v126, v126, v130, v134 op_sel_hi:[1,0,0]\n\t"
        "v_cvt_pk_bf16_f32 v136, v98, v102\n\t"
        "v_cvt_pk_bf16_f32 v137, v106, v110\n\t"
        "v_cvt_pk_bf16_f32 v138, v114, v118\n\t"
        "v_cvt_pk_bf16_f32 v139, v122, v126\n\t"
        "v_xor_b32 v164, s90, v147\n\t"
        "ds_write_b128 v164, v[136:139] offset:2048\n\t"
        "v_fma_mix_f32 v99, v99, v131, v135 op_sel_hi:[1,0,0]\n\t"
        "v_fma_mix_f32 v103, v103, v131, v135 op_sel_hi:[1,0,0]\n\t"
        "v_fma_mix_f32 v107, v107, v131, v135 op_sel_hi:[1,0,0]\n\t"
        "v_fma_mix_f32 v111, v111, v131, v135 op_sel_hi:[1,0,0]\n\t"
        "v_fma_mix_f32 v115, v115, v131, v135 op_sel_hi:[1,0,0]\n\t"
        "v_fma_mix_f32 v119, v119, v131, v135 op_sel_hi:[1,0,0]\n\t"
        "v_fma_mix_f32 v123, v123, v131, v135 op_sel_hi:[1,0,0]\n\t"
        "v_fma_mix_f32 v127, v127, v131, v135 op_sel_hi:[1,0,0]\n\t"
        "v_cvt_pk_bf16_f32 v140, v99, v103\n\t"
        "v_cvt_pk_bf16_f32 v141, v107, v111\n\t"
        "v_cvt_pk_bf16_f32 v142, v115, v119\n\t"
        "v_cvt_pk_bf16_f32 v143, v123, v127\n\t"
        "v_xor_b32 v164, s91, v147\n\t"
        "ds_write_b128 v164, v[140:143] offset:3072\n\t"
        "s_waitcnt lgkmcnt(0)\n\t"
        "s_cmp_lt_u32 s48, 2\n\t"
        "s_cbranch_scc0 3f\n\t"
        "s_barrier\n\t"
        "3:\n\t"
        "s_branch 6f\n\t"
        "8:\n\t"
        "s_sub_u32 s56, s48, 4\n\t"
        "s_lshl_b32 s59, s50, 3\n\t"
        "s_add_u32 s59, s59, s56\n\t"
        "s_mul_i32 s57, s56, 8320\n\t"
        "s_add_u32 s57, s57, 46592\n\t"
        "s_mul_i32 s58, s59, 36864\n\t"
        "s_mul_i32 s89, s59, 294912\n\t"
        "s_mul_i32 s90, s51, 1152\n\t"
        "s_add_u32 s89, s89, s90\n\t"
        "s_mov_b32 s26, 36864\n\t"
        "s_mov_b32 s27, 0x00020000\n\t"
        "s_mov_b32 s38, 286848\n\t"
        "s_mov_b32 s39, 0x00020000\n\t"
        "s_bitcmp1_b32 s49, 0\n\t"
        "s_cselect_b32 s54, 18432, 0\n\t"
        "s_cselect_b32 s55, 0, 18432\n\t"
        "v_lshrrev_b32 v165, 7, v166\n\t"
        "v_mul_u32_u24 v162, 0x480, v165\n\t"
        "v_lshrrev_b32 v165, 4, v166\n\t"
        "v_xor_b32 v165, v165, v166\n\t"
        "v_and_b32 v164, 0x30, v165\n\t"
        "v_and_b32 v165, 0x40, v166\n\t"
        "v_add3_u32 v162, v162, v165, v164\n\t"
        "s_waitcnt lgkmcnt(0)\n\t"
        "s_add_u32 s24, s8, s58\n\t"
        "s_addc_u32 s25, s9, 0\n\t"
        "s_add_u32 s36, s16, s89\n\t"
        "s_addc_u32 s37, s17, 0\n\t"
        "s_bfe_u32 s15, s84, 0x40000\n\t"
        "s_lshl_b32 s15, s15, 7\n\t"
        "s_add_u32 s14, s15, s54\n\t"
        "s_add_u32 m0, s57, 0\n\t"
        "s_add_u32 s59, s14, 0\n\t"
        "buffer_load_dwordx4 v162, s[24:27], s59 offen lds\n\t"
        "s_add_u32 m0, s57, 4160\n\t"
        "s_add_u32 s59, s14, 9216\n\t"
        "buffer_load_dwordx4 v162, s[24:27], s59 offen lds\n\t"
        "s_add_u32 s14, s15, s55\n\t"
        "s_add_u32 m0, s57, 1024\n\t"
        "s_add_u32 s59, s14, 0\n\t"
        "buffer_load_dwordx4 v162, s[24:27], s59 offen lds\n\t"
        "s_add_u32 m0, s57, 5184\n\t"
        "s_add_u32 s59, s14, 9216\n\t"
        "buffer_load_dwordx4 v162, s[24:27], s59 offen lds\n\t"
        "s_bfe_u32 s15, s84, 0x40004\n\t"
        "s_lshl_b32 s15, s15, 7\n\t"
        "s_add_u32 s14, s15, s54\n\t"
        "s_add_u32 m0, s57, 2048\n\t"
        "s_add_u32 s59, s14, 0\n\t"
        "buffer_load_dwordx4 v162, s[24:27], s59 offen lds\n\t"
        "s_add_u32 m0, s57, 6208\n\t"
        "s_add_u32 s59, s14, 9216\n\t"
        "buffer_load_dwordx4 v162, s[24:27], s59 offen lds\n\t"
        "s_getpc_b64 s[44:45]\n\t"
        "5:\n\t"
        "s_mov_b32 s46, 6f-5b\n\t"
        "s_mov_b32 s47, 0x00020000\n\t"
        "s_and_b32 s59, s50, 3\n\t"
        "s_lshl_b32 s59, s59, 3\n\t"
        "s_add_u32 s59, s59, s56\n\t"
        "s_lshl_b32 s59, s59, 9\n\t"
        "v_lshlrev_b32 v165, 3, v166\n\t"
        "v_and_b32 v164, 0x3c0, v166\n\t"
        "v_lshl_add_u32 v165, v164, 20, v165\n\t"
        "buffer_load_dword v163, v165, s[44:47], s59 offen\n\t"
        "v_bfe_u32 v165, v166, 7, 1\n\t"
        "v_mul_u32_u24 v165, 4160, v165\n\t"
        "v_and_b32 v164, 0x70, v166\n\t"
        "v_lshl_add_u32 v165, v164, 3, v165\n\t"
        "v_lshrrev_b32 v164, 1, v166\n\t"
        "v_lshrrev_b32 v161, 4, v166\n\t"
        "v_xor_b32 v164, v164, v161\n\t"
        "v_and_b32 v164, 0x30, v164\n\t"
        "v_add3_u32 v161, v165, v164, s57\n\t"
        "v_bfe_u32 v165, v166, 4, 2\n\t"
        "v_lshlrev_b32 v160, 10, v165\n\t"
        "v_bfe_u32 v164, v166, 6, 2\n\t"
        "v_xor_b32 v165, v165, v164\n\t"
        "v_lshl_add_u32 v160, v165, 5, v160\n\t"
        "v_lshrrev_b32 v165, 8, v166\n\t"
        "v_lshl_add_u32 v160, v165, 7, v160\n\t"
        "v_add_u32 v167, s57, v166\n\t"
        "s_lshl_b32 s90, s54, 3\n\t"
        "s_lshl_b32 s91, s55, 3\n\t"
        "s_waitcnt vmcnt(5)\n\t"
        "ds_read_b128 v[0:3], v161 offset:0\n\t"
        "ds_read_b128 v[8:11], v161 offset:64\n\t"
        "s_waitcnt vmcnt(3)\n\t"
        "ds_read_b128 v[4:7], v161 offset:1024\n\t"
        "ds_read_b128 v[12:15], v161 offset:1088\n\t"
        "s_barrier\n\t"
        "ds_read_b128 v[136:139], v166 offset:0\n\t"
        "ds_read_b128 v[140:143], v166 offset:1024\n\t"
        "ds_read_b128 v[144:147], v166 offset:2048\n\t"
        "ds_read_b128 v[148:151], v166 offset:3072\n\t"
        "ds_read_b128 v[152:155], v160 offset:4096\n\t"
        "ds_read_b128 v[156:159], v160 offset:4112\n\t"
        "s_add_u32 s14, s15, s55\n\t"
        "s_add_u32 m0, s57, 3072\n\t"
        "s_add_u32 s59, s14, 0\n\t"
        "buffer_load_dwordx4 v162, s[24:27], s59 offen lds\n\t"
        "s_setprio 1\n\t"
        "s_setprio 0\n\t"
        "s_waitcnt lgkmcnt(5)\n\t"
        "v_mfma_f32_16x16x32_bf16 v[64:67], v[0:3], v[136:139], 0\n\t"
        "s_barrier\n\t"
        "s_add_u32 m0, s57, 7232\n\t"
        "s_add_u32 s59, s14, 9216\n\t"
        "buffer_load_dwordx4 v162, s[24:27], s59 offen lds\n\t"
        "s_setprio 1\n\t"
        "v_mfma_f32_16x16x32_bf16 v[80:83], v[4:7], v[136:139], 0\n\t"
        "ds_read_b128 v[136:139], v166 offset:8192\n\t"
        "s_waitcnt lgkmcnt(5)\n\t"
        "v_mfma_f32_16x16x32_bf16 v[68:71], v[0:3], v[140:143], 0\n\t"
        "v_mfma_f32_16x16x32_bf16 v[84:87], v[4:7], v[140:143], 0\n\t"
        "ds_read_b128 v[140:143], v166 offset:9216\n\t"
        "s_waitcnt lgkmcnt(5)\n\t"
        "v_mfma_f32_16x16x32_bf16 v[72:75], v[0:3], v[144:147], 0\n\t"
        "v_mfma_f32_16x16x32_bf16 v[88:91], v[4:7], v[144:147], 0\n\t"
        "ds_read_b128 v[144:147], v166 offset:10240\n\t"
        "s_waitcnt lgkmcnt(5)\n\t"
        "v_mfma_f32_16x16x32_bf16 v[76:79], v[0:3], v[148:151], 0\n\t"
        "v_mfma_f32_16x16x32_bf16 v[92:95], v[4:7], v[148:151], 0\n\t"
        "ds_read_b128 v[148:151], v166 offset:11264\n\t"
        "s_setprio 0\n\t"
        "s_waitcnt lgkmcnt(5)\n\t"
        "v_mfma_f32_16x16x32_bf16 v[128:131], v[152:155], v[0:3], 0\n\t"
        "ds_read_b128 v[152:155], v166 offset:12288\n\t"
        "s_waitcnt lgkmcnt(5)\n\t"
        "v_mfma_f32_16x16x32_bf16 v[132:135], v[156:159], v[0:3], 0\n\t"
        "ds_read_b128 v[156:159], v166 offset:13312\n\t"
        "s_waitcnt lgkmcnt(5)\n\t"
        "v_mfma_f32_16x16x32_bf16 v[96:99], v[0:3], v[136:139], 0\n\t"
        "v_mfma_f32_16x16x32_bf16 v[112:115], v[4:7], v[136:139], 0\n\t"
        "ds_read_b128 v[136:139], v166 offset:14336\n\t"
        "s_waitcnt lgkmcnt(5)\n\t"
        "v_mfma_f32_16x16x32_bf16 v[100:103], v[0:3], v[140:143], 0\n\t"
        "s_bfe_u32 s15, s84, 0x40008\n\t"
        "s_lshl_b32 s15, s15, 7\n\t"
        "s_add_u32 s14, s15, s54\n\t"
        "s_add_u32 m0, s57, 0\n\t"
        "s_add_u32 s59, s14, 0\n\t"
        "buffer_load_dwordx4 v162, s[24:27], s59 offen lds\n\t"
        "v_mfma_f32_16x16x32_bf16 v[116:119], v[4:7], v[140:143], 0\n\t"
        "ds_read_b128 v[140:143], v166 offset:15360\n\t"
        "s_add_u32 m0, s57, 4160\n\t"
        "s_add_u32 s59, s14, 9216\n\t"
        "buffer_load_dwordx4 v162, s[24:27], s59 offen lds\n\t"
        "s_waitcnt lgkmcnt(5)\n\t"
        "v_mfma_f32_16x16x32_bf16 v[104:107], v[0:3], v[144:147], 0\n\t"
        "s_barrier\n\t"
        "s_setprio 1\n\t"
        "v_mfma_f32_16x16x32_bf16 v[120:123], v[4:7], v[144:147], 0\n\t"
        "ds_read_b128 v[144:147], v160 offset:4608\n\t"
        "s_waitcnt lgkmcnt(5)\n\t"
        "v_mfma_f32_16x16x32_bf16 v[108:111], v[0:3], v[148:151], 0\n\t"
        "s_waitcnt vmcnt(5)\n\t"
        "ds_read_b128 v[16:19], v161 offset:2048\n\t"
        "ds_read_b128 v[24:27], v161 offset:2112\n\t"
        "v_mfma_f32_16x16x32_bf16 v[124:127], v[4:7], v[148:151], 0\n\t"
        "ds_read_b128 v[148:151], v160 offset:4624\n\t"
        "s_waitcnt lgkmcnt(7)\n\t"
        "v_mfma_f32_16x16x32_bf16 v[64:67], v[8:11], v[152:155], v[64:67]\n\t"
        "v_mfma_f32_16x16x32_bf16 v[80:83], v[12:15], v[152:155], v[80:83]\n\t"
        "ds_read_b128 v[152:155], v166 offset:16384\n\t"
        "s_waitcnt lgkmcnt(7)\n\t"
        "v_mfma_f32_16x16x32_bf16 v[68:71], v[8:11], v[156:159], v[68:71]\n\t"
        "v_mfma_f32_16x16x32_bf16 v[84:87], v[12:15], v[156:159], v[84:87]\n\t"
        "ds_read_b128 v[156:159], v166 offset:17408\n\t"
        "s_waitcnt lgkmcnt(7)\n\t"
        "v_mfma_f32_16x16x32_bf16 v[72:75], v[8:11], v[136:139], v[72:75]\n\t"
        "s_setprio 0\n\t"
        "v_mfma_f32_16x16x32_bf16 v[88:91], v[12:15], v[136:139], v[88:91]\n\t"
        "ds_read_b128 v[136:139], v166 offset:18432\n\t"
        "s_waitcnt lgkmcnt(7)\n\t"
        "v_mfma_f32_16x16x32_bf16 v[76:79], v[8:11], v[140:143], v[76:79]\n\t"
        "v_mfma_f32_16x16x32_bf16 v[92:95], v[12:15], v[140:143], v[92:95]\n\t"
        "ds_read_b128 v[140:143], v166 offset:19456\n\t"
        "s_waitcnt lgkmcnt(7)\n\t"
        "v_mfma_f32_16x16x32_bf16 v[128:131], v[144:147], v[8:11], v[128:131]\n\t"
        "ds_read_b128 v[144:147], v166 offset:20480\n\t"
        "s_waitcnt lgkmcnt(5)\n\t"
        "v_mfma_f32_16x16x32_bf16 v[132:135], v[148:151], v[8:11], v[132:135]\n\t"
        "ds_read_b128 v[148:151], v166 offset:21504\n\t"
        "s_waitcnt lgkmcnt(5)\n\t"
        "v_mfma_f32_16x16x32_bf16 v[96:99], v[8:11], v[152:155], v[96:99]\n\t"
        "v_mfma_f32_16x16x32_bf16 v[112:115], v[12:15], v[152:155], v[112:115]\n\t"
        "ds_read_b128 v[152:155], v166 offset:22528\n\t"
        "s_add_u32 s14, s15, s55\n\t"
        "s_add_u32 m0, s57, 1024\n\t"
        "s_add_u32 s59, s14, 0\n\t"
        "buffer_load_dwordx4 v162, s[24:27], s59 offen lds\n\t"
        "s_waitcnt lgkmcnt(5)\n\t"
        "v_mfma_f32_16x16x32_bf16 v[100:103], v[8:11], v[156:159], v[100:103]\n\t"
        "s_barrier\n\t"
        "s_add_u32 m0, s57, 5184\n\t"
        "s_add_u32 s59, s14, 9216\n\t"
        "buffer_load_dwordx4 v162, s[24:27], s59 offen lds\n\t"
        "s_setprio 1\n\t"
        "v_mfma_f32_16x16x32_bf16 v[116:119], v[12:15], v[156:159], v[116:119]\n\t"
        "ds_read_b128 v[156:159], v166 offset:23552\n\t"
        "s_waitcnt lgkmcnt(5)\n\t"
        "v_mfma_f32_16x16x32_bf16 v[104:107], v[8:11], v[136:139], v[104:107]\n\t"
        "v_mfma_f32_16x16x32_bf16 v[120:123], v[12:15], v[136:139], v[120:123]\n\t"
        "ds_read_b128 v[136:139], v160 offset:24576\n\t"
        "s_waitcnt vmcnt(4)\n\t"
        "ds_read_b128 v[20:23], v161 offset:3072\n\t"
        "ds_read_b128 v[28:31], v161 offset:3136\n\t"
        "s_waitcnt lgkmcnt(7)\n\t"
        "v_mfma_f32_16x16x32_bf16 v[108:111], v[8:11], v[140:143], v[108:111]\n\t"
        "v_mfma_f32_16x16x32_bf16 v[124:127], v[12:15], v[140:143], v[124:127]\n\t"
        "ds_read_b128 v[140:143], v160 offset:24592\n\t"
        "s_setprio 0\n\t"
        "s_waitcnt lgkmcnt(7)\n\t"
        "v_mfma_f32_16x16x32_bf16 v[64:67], v[16:19], v[144:147], v[64:67]\n\t"
        "s_waitcnt lgkmcnt(2)\n\t"
        "v_mfma_f32_16x16x32_bf16 v[80:83], v[20:23], v[144:147], v[80:83]\n\t"
        "ds_read_b128 v[144:147], v166 offset:28672\n\t"
        "v_mfma_f32_16x16x32_bf16 v[68:71], v[16:19], v[148:151], v[68:71]\n\t"
        "v_mfma_f32_16x16x32_bf16 v[84:87], v[20:23], v[148:151], v[84:87]\n\t"
        "ds_read_b128 v[148:151], v166 offset:29696\n\t"
        "v_mfma_f32_16x16x32_bf16 v[72:75], v[16:19], v[152:155], v[72:75]\n\t"
        "s_barrier\n\t"
        "s_setprio 1\n\t"
        "v_mfma_f32_16x16x32_bf16 v[88:91], v[20:23], v[152:155], v[88:91]\n\t"
        "ds_read_b128 v[152:155], v166 offset:30720\n\t"
        "v_mfma_f32_16x16x32_bf16 v[76:79], v[16:19], v[156:159], v[76:79]\n\t"
        "v_mfma_f32_16x16x32_bf16 v[92:95], v[20:23], v[156:159], v[92:95]\n\t"
        "ds_read_b128 v[156:159], v166 offset:31744\n\t"
        "v_mfma_f32_16x16x32_bf16 v[128:131], v[136:139], v[16:19], v[128:131]\n\t"
        "ds_read_b128 v[136:139], v166 offset:0\n\t"
        "s_waitcnt lgkmcnt(5)\n\t"
        "v_mfma_f32_16x16x32_bf16 v[132:135], v[140:143], v[16:19], v[132:135]\n\t"
        "ds_read_b128 v[140:143], v166 offset:1024\n\t"
        "s_waitcnt lgkmcnt(5)\n\t"
        "v_mfma_f32_16x16x32_bf16 v[96:99], v[16:19], v[144:147], v[96:99]\n\t"
        "v_mfma_f32_16x16x32_bf16 v[112:115], v[20:23], v[144:147], v[112:115]\n\t"
        "ds_read_b128 v[144:147], v166 offset:2048\n\t"
        "s_waitcnt lgkmcnt(5)\n\t"
        "v_mfma_f32_16x16x32_bf16 v[100:103], v[16:19], v[148:151], v[100:103]\n\t"
        "s_bfe_u32 s15, s84, 0x4000c\n\t"
        "s_lshl_b32 s15, s15, 7\n\t"
        "s_add_u32 s14, s15, s54\n\t"
        "s_add_u32 m0, s57, 2048\n\t"
        "s_add_u32 s59, s14, 0\n\t"
        "buffer_load_dwordx4 v162, s[24:27], s59 offen lds\n\t"
        "v_mfma_f32_16x16x32_bf16 v[116:119], v[20:23], v[148:151], v[116:119]\n\t"
        "ds_read_b128 v[148:151], v166 offset:3072\n\t"
        "s_add_u32 m0, s57, 6208\n\t"
        "s_add_u32 s59, s14, 9216\n\t"
        "buffer_load_dwordx4 v162, s[24:27], s59 offen lds\n\t"
        "s_setprio 0\n\t"
        "s_waitcnt lgkmcnt(5)\n\t"
        "v_mfma_f32_16x16x32_bf16 v[104:107], v[16:19], v[152:155], v[104:107]\n\t"
        "v_mfma_f32_16x16x32_bf16 v[120:123], v[20:23], v[152:155], v[120:123]\n\t"
        "ds_read_b128 v[152:155], v160 offset:25088\n\t"
        "s_waitcnt lgkmcnt(5)\n\t"
        "v_mfma_f32_16x16x32_bf16 v[108:111], v[16:19], v[156:159], v[108:111]\n\t"
        "s_waitcnt vmcnt(4)\n\t"
        "ds_read_b128 v[32:35], v161 offset:0\n\t"
        "ds_read_b128 v[40:43], v161 offset:64\n\t"
        "v_mfma_f32_16x16x32_bf16 v[124:127], v[20:23], v[156:159], v[124:127]\n\t"
        "ds_read_b128 v[156:159], v160 offset:25104\n\t"
        "s_waitcnt lgkmcnt(7)\n\t"
        "v_mfma_f32_16x16x32_bf16 v[64:67], v[24:27], v[136:139], v[64:67]\n\t"
        "v_mfma_f32_16x16x32_bf16 v[80:83], v[28:31], v[136:139], v[80:83]\n\t"
        "ds_read_b128 v[136:139], v166 offset:32768\n\t"
        "s_waitcnt lgkmcnt(7)\n\t"
        "v_mfma_f32_16x16x32_bf16 v[68:71], v[24:27], v[140:143], v[68:71]\n\t"
        "v_mfma_f32_16x16x32_bf16 v[84:87], v[28:31], v[140:143], v[84:87]\n\t"
        "ds_read_b128 v[140:143], v166 offset:33792\n\t"
        "s_waitcnt lgkmcnt(7)\n\t"
        "v_mfma_f32_16x16x32_bf16 v[72:75], v[24:27], v[144:147], v[72:75]\n\t"
        "s_barrier\n\t"
        "s_setprio 1\n\t"
        "v_mfma_f32_16x16x32_bf16 v[88:91], v[28:31], v[144:147], v[88:91]\n\t"
        "ds_read_b128 v[144:147], v166 offset:34816\n\t"
        "s_waitcnt lgkmcnt(7)\n\t"
        "v_mfma_f32_16x16x32_bf16 v[76:79], v[24:27], v[148:151], v[76:79]\n\t"
        "v_mfma_f32_16x16x32_bf16 v[92:95], v[28:31], v[148:151], v[92:95]\n\t"
        "ds_read_b128 v[148:151], v166 offset:35840\n\t"
        "s_waitcnt lgkmcnt(7)\n\t"
        "v_mfma_f32_16x16x32_bf16 v[128:131], v[152:155], v[24:27], v[128:131]\n\t"
        "ds_read_b128 v[152:155], v166 offset:4096\n\t"
        "s_waitcnt lgkmcnt(5)\n\t"
        "v_mfma_f32_16x16x32_bf16 v[132:135], v[156:159], v[24:27], v[132:135]\n\t"
        "ds_read_b128 v[156:159], v166 offset:5120\n\t"
        "s_setprio 0\n\t"
        "s_waitcnt lgkmcnt(5)\n\t"
        "v_mfma_f32_16x16x32_bf16 v[96:99], v[24:27], v[136:139], v[96:99]\n\t"
        "v_mfma_f32_16x16x32_bf16 v[112:115], v[28:31], v[136:139], v[112:115]\n\t"
        "ds_read_b128 v[136:139], v166 offset:6144\n\t"
        "s_add_u32 s14, s15, s55\n\t"
        "s_add_u32 m0, s57, 3072\n\t"
        "s_add_u32 s59, s14, 0\n\t"
        "buffer_load_dwordx4 v162, s[24:27], s59 offen lds\n\t"
        "s_waitcnt lgkmcnt(5)\n\t"
        "v_mfma_f32_16x16x32_bf16 v[100:103], v[24:27], v[140:143], v[100:103]\n\t"
        "s_add_u32 m0, s57, 7232\n\t"
        "s_add_u32 s59, s14, 9216\n\t"
        "buffer_load_dwordx4 v162, s[24:27], s59 offen lds\n\t"
        "v_mfma_f32_16x16x32_bf16 v[116:119], v[28:31], v[140:143], v[116:119]\n\t"
        "ds_read_b128 v[140:143], v166 offset:7168\n\t"
        "s_waitcnt lgkmcnt(5)\n\t"
        "v_mfma_f32_16x16x32_bf16 v[104:107], v[24:27], v[144:147], v[104:107]\n\t"
        "s_barrier\n\t"
        "s_setprio 1\n\t"
        "v_mfma_f32_16x16x32_bf16 v[120:123], v[28:31], v[144:147], v[120:123]\n\t"
        "ds_read_b128 v[144:147], v160 offset:8192\n\t"
        "s_waitcnt vmcnt(4)\n\t"
        "ds_read_b128 v[36:39], v161 offset:1024\n\t"
        "ds_read_b128 v[44:47], v161 offset:1088\n\t"
        "s_waitcnt lgkmcnt(7)\n\t"
        "v_mfma_f32_16x16x32_bf16 v[108:111], v[24:27], v[148:151], v[108:111]\n\t"
        "v_mfma_f32_16x16x32_bf16 v[124:127], v[28:31], v[148:151], v[124:127]\n\t"
        "ds_read_b128 v[148:151], v160 offset:8208\n\t"
        "s_waitcnt lgkmcnt(7)\n\t"
        "v_mfma_f32_16x16x32_bf16 v[64:67], v[32:35], v[152:155], v[64:67]\n\t"
        "s_waitcnt lgkmcnt(2)\n\t"
        "v_mfma_f32_16x16x32_bf16 v[80:83], v[36:39], v[152:155], v[80:83]\n\t"
        "ds_read_b128 v[152:155], v166 offset:12288\n\t"
        "v_mfma_f32_16x16x32_bf16 v[68:71], v[32:35], v[156:159], v[68:71]\n\t"
        "v_mfma_f32_16x16x32_bf16 v[84:87], v[36:39], v[156:159], v[84:87]\n\t"
        "ds_read_b128 v[156:159], v166 offset:13312\n\t"
        "v_mfma_f32_16x16x32_bf16 v[72:75], v[32:35], v[136:139], v[72:75]\n\t"
        "v_mfma_f32_16x16x32_bf16 v[88:91], v[36:39], v[136:139], v[88:91]\n\t"
        "ds_read_b128 v[136:139], v166 offset:14336\n\t"
        "s_setprio 0\n\t"
        "v_mfma_f32_16x16x32_bf16 v[76:79], v[32:35], v[140:143], v[76:79]\n\t"
        "v_mfma_f32_16x16x32_bf16 v[92:95], v[36:39], v[140:143], v[92:95]\n\t"
        "ds_read_b128 v[140:143], v166 offset:15360\n\t"
        "v_mfma_f32_16x16x32_bf16 v[128:131], v[144:147], v[32:35], v[128:131]\n\t"
        "ds_read_b128 v[144:147], v166 offset:16384\n\t"
        "s_waitcnt lgkmcnt(5)\n\t"
        "v_mfma_f32_16x16x32_bf16 v[132:135], v[148:151], v[32:35], v[132:135]\n\t"
        "ds_read_b128 v[148:151], v166 offset:17408\n\t"
        "s_waitcnt lgkmcnt(5)\n\t"
        "v_mfma_f32_16x16x32_bf16 v[96:99], v[32:35], v[152:155], v[96:99]\n\t"
        "v_mfma_f32_16x16x32_bf16 v[112:115], v[36:39], v[152:155], v[112:115]\n\t"
        "ds_read_b128 v[152:155], v166 offset:18432\n\t"
        "s_waitcnt lgkmcnt(5)\n\t"
        "v_mfma_f32_16x16x32_bf16 v[100:103], v[32:35], v[156:159], v[100:103]\n\t"
        "s_bfe_u32 s15, s84, 0x40010\n\t"
        "s_lshl_b32 s15, s15, 7\n\t"
        "s_add_u32 s14, s15, s54\n\t"
        "s_add_u32 m0, s57, 0\n\t"
        "s_add_u32 s59, s14, 0\n\t"
        "buffer_load_dwordx4 v162, s[24:27], s59 offen lds\n\t"
        "v_mfma_f32_16x16x32_bf16 v[116:119], v[36:39], v[156:159], v[116:119]\n\t"
        "ds_read_b128 v[156:159], v166 offset:19456\n\t"
        "s_add_u32 m0, s57, 4160\n\t"
        "s_add_u32 s59, s14, 9216\n\t"
        "buffer_load_dwordx4 v162, s[24:27], s59 offen lds\n\t"
        "s_waitcnt lgkmcnt(5)\n\t"
        "v_mfma_f32_16x16x32_bf16 v[104:107], v[32:35], v[136:139], v[104:107]\n\t"
        "s_barrier\n\t"
        "s_setprio 1\n\t"
        "v_mfma_f32_16x16x32_bf16 v[120:123], v[36:39], v[136:139], v[120:123]\n\t"
        "ds_read_b128 v[136:139], v160 offset:8704\n\t"
        "s_waitcnt lgkmcnt(5)\n\t"
        "v_mfma_f32_16x16x32_bf16 v[108:111], v[32:35], v[140:143], v[108:111]\n\t"
        "s_waitcnt vmcnt(4)\n\t"
        "ds_read_b128 v[48:51], v161 offset:2048\n\t"
        "ds_read_b128 v[56:59], v161 offset:2112\n\t"
        "v_mfma_f32_16x16x32_bf16 v[124:127], v[36:39], v[140:143], v[124:127]\n\t"
        "ds_read_b128 v[140:143], v160 offset:8720\n\t"
        "s_waitcnt lgkmcnt(7)\n\t"
        "v_mfma_f32_16x16x32_bf16 v[64:67], v[40:43], v[144:147], v[64:67]\n\t"
        "v_mfma_f32_16x16x32_bf16 v[80:83], v[44:47], v[144:147], v[80:83]\n\t"
        "ds_read_b128 v[144:147], v166 offset:20480\n\t"
        "s_waitcnt lgkmcnt(7)\n\t"
        "v_mfma_f32_16x16x32_bf16 v[68:71], v[40:43], v[148:151], v[68:71]\n\t"
        "v_mfma_f32_16x16x32_bf16 v[84:87], v[44:47], v[148:151], v[84:87]\n\t"
        "ds_read_b128 v[148:151], v166 offset:21504\n\t"
        "s_waitcnt lgkmcnt(7)\n\t"
        "v_mfma_f32_16x16x32_bf16 v[72:75], v[40:43], v[152:155], v[72:75]\n\t"
        "s_setprio 0\n\t"
        "v_mfma_f32_16x16x32_bf16 v[88:91], v[44:47], v[152:155], v[88:91]\n\t"
        "ds_read_b128 v[152:155], v166 offset:22528\n\t"
        "s_waitcnt lgkmcnt(7)\n\t"
        "v_mfma_f32_16x16x32_bf16 v[76:79], v[40:43], v[156:159], v[76:79]\n\t"
        "v_mfma_f32_16x16x32_bf16 v[92:95], v[44:47], v[156:159], v[92:95]\n\t"
        "ds_read_b128 v[156:159], v166 offset:23552\n\t"
        "s_waitcnt lgkmcnt(7)\n\t"
        "v_mfma_f32_16x16x32_bf16 v[128:131], v[136:139], v[40:43], v[128:131]\n\t"
        "ds_read_b128 v[136:139], v166 offset:28672\n\t"
        "s_waitcnt lgkmcnt(5)\n\t"
        "v_mfma_f32_16x16x32_bf16 v[132:135], v[140:143], v[40:43], v[132:135]\n\t"
        "ds_read_b128 v[140:143], v166 offset:29696\n\t"
        "s_waitcnt lgkmcnt(5)\n\t"
        "v_mfma_f32_16x16x32_bf16 v[96:99], v[40:43], v[144:147], v[96:99]\n\t"
        "v_mfma_f32_16x16x32_bf16 v[112:115], v[44:47], v[144:147], v[112:115]\n\t"
        "ds_read_b128 v[144:147], v166 offset:30720\n\t"
        "s_add_u32 s14, s15, s55\n\t"
        "s_add_u32 m0, s57, 1024\n\t"
        "s_add_u32 s59, s14, 0\n\t"
        "buffer_load_dwordx4 v162, s[24:27], s59 offen lds\n\t"
        "s_waitcnt lgkmcnt(5)\n\t"
        "v_mfma_f32_16x16x32_bf16 v[100:103], v[40:43], v[148:151], v[100:103]\n\t"
        "s_barrier\n\t"
        "s_add_u32 m0, s57, 5184\n\t"
        "s_add_u32 s59, s14, 9216\n\t"
        "buffer_load_dwordx4 v162, s[24:27], s59 offen lds\n\t"
        "s_setprio 1\n\t"
        "v_mfma_f32_16x16x32_bf16 v[116:119], v[44:47], v[148:151], v[116:119]\n\t"
        "ds_read_b128 v[148:151], v166 offset:31744\n\t"
        "s_waitcnt lgkmcnt(5)\n\t"
        "v_mfma_f32_16x16x32_bf16 v[104:107], v[40:43], v[152:155], v[104:107]\n\t"
        "v_mfma_f32_16x16x32_bf16 v[120:123], v[44:47], v[152:155], v[120:123]\n\t"
        "ds_read_b128 v[152:155], v160 offset:0\n\t"
        "s_waitcnt vmcnt(4)\n\t"
        "ds_read_b128 v[52:55], v161 offset:3072\n\t"
        "ds_read_b128 v[60:63], v161 offset:3136\n\t"
        "s_waitcnt lgkmcnt(7)\n\t"
        "v_mfma_f32_16x16x32_bf16 v[108:111], v[40:43], v[156:159], v[108:111]\n\t"
        "v_mfma_f32_16x16x32_bf16 v[124:127], v[44:47], v[156:159], v[124:127]\n\t"
        "ds_read_b128 v[156:159], v160 offset:16\n\t"
        "s_setprio 0\n\t"
        "s_waitcnt lgkmcnt(7)\n\t"
        "v_mfma_f32_16x16x32_bf16 v[64:67], v[48:51], v[136:139], v[64:67]\n\t"
        "s_waitcnt lgkmcnt(2)\n\t"
        "v_mfma_f32_16x16x32_bf16 v[80:83], v[52:55], v[136:139], v[80:83]\n\t"
        "ds_read_b128 v[136:139], v166 offset:24576\n\t"
        "v_mfma_f32_16x16x32_bf16 v[68:71], v[48:51], v[140:143], v[68:71]\n\t"
        "v_mfma_f32_16x16x32_bf16 v[84:87], v[52:55], v[140:143], v[84:87]\n\t"
        "ds_read_b128 v[140:143], v166 offset:25600\n\t"
        "v_mfma_f32_16x16x32_bf16 v[72:75], v[48:51], v[144:147], v[72:75]\n\t"
        "s_barrier\n\t"
        "s_setprio 1\n\t"
        "v_mfma_f32_16x16x32_bf16 v[88:91], v[52:55], v[144:147], v[88:91]\n\t"
        "ds_read_b128 v[144:147], v166 offset:26624\n\t"
        "v_mfma_f32_16x16x32_bf16 v[76:79], v[48:51], v[148:151], v[76:79]\n\t"
        "v_mfma_f32_16x16x32_bf16 v[92:95], v[52:55], v[148:151], v[92:95]\n\t"
        "ds_read_b128 v[148:151], v166 offset:27648\n\t"
        "v_mfma_f32_16x16x32_bf16 v[128:131], v[152:155], v[48:51], v[128:131]\n\t"
        "ds_read_b128 v[152:155], v166 offset:4096\n\t"
        "s_waitcnt lgkmcnt(5)\n\t"
        "v_mfma_f32_16x16x32_bf16 v[132:135], v[156:159], v[48:51], v[132:135]\n\t"
        "ds_read_b128 v[156:159], v166 offset:5120\n\t"
        "s_waitcnt lgkmcnt(5)\n\t"
        "v_mfma_f32_16x16x32_bf16 v[96:99], v[48:51], v[136:139], v[96:99]\n\t"
        "v_mfma_f32_16x16x32_bf16 v[112:115], v[52:55], v[136:139], v[112:115]\n\t"
        "ds_read_b128 v[136:139], v166 offset:6144\n\t"
        "s_waitcnt lgkmcnt(5)\n\t"
        "v_mfma_f32_16x16x32_bf16 v[100:103], v[48:51], v[140:143], v[100:103]\n\t"
        "s_bfe_u32 s15, s84, 0x40014\n\t"
        "s_lshl_b32 s15, s15, 7\n\t"
        "s_add_u32 s14, s15, s54\n\t"
        "s_add_u32 m0, s57, 2048\n\t"
        "s_add_u32 s59, s14, 0\n\t"
        "buffer_load_dwordx4 v162, s[24:27], s59 offen lds\n\t"
        "v_mfma_f32_16x16x32_bf16 v[116:119], v[52:55], v[140:143], v[116:119]\n\t"
        "ds_read_b128 v[140:143], v166 offset:7168\n\t"
        "s_add_u32 m0, s57, 6208\n\t"
        "s_add_u32 s59, s14, 9216\n\t"
        "buffer_load_dwordx4 v162, s[24:27], s59 offen lds\n\t"
        "s_setprio 0\n\t"
        "s_waitcnt lgkmcnt(5)\n\t"
        "v_mfma_f32_16x16x32_bf16 v[104:107], v[48:51], v[144:147], v[104:107]\n\t"
        "v_mfma_f32_16x16x32_bf16 v[120:123], v[52:55], v[144:147], v[120:123]\n\t"
        "ds_read_b128 v[144:147], v160 offset:512\n\t"
        "s_waitcnt lgkmcnt(5)\n\t"
        "v_mfma_f32_16x16x32_bf16 v[108:111], v[48:51], v[148:151], v[108:111]\n\t"
        "s_waitcnt vmcnt(4)\n\t"
        "ds_read_b128 v[0:3], v161 offset:0\n\t"
        "ds_read_b128 v[8:11], v161 offset:64\n\t"
        "v_mfma_f32_16x16x32_bf16 v[124:127], v[52:55], v[148:151], v[124:127]\n\t"
        "ds_read_b128 v[148:151], v160 offset:528\n\t"
        "s_waitcnt lgkmcnt(7)\n\t"
        "v_mfma_f32_16x16x32_bf16 v[64:67], v[56:59], v[152:155], v[64:67]\n\t"
        "v_mfma_f32_16x16x32_bf16 v[80:83], v[60:63], v[152:155], v[80:83]\n\t"
        "ds_read_b128 v[152:155], v166 offset:32768\n\t"
        "s_waitcnt lgkmcnt(7)\n\t"
        "v_mfma_f32_16x16x32_bf16 v[68:71], v[56:59], v[156:159], v[68:71]\n\t"
        "v_mfma_f32_16x16x32_bf16 v[84:87], v[60:63], v[156:159], v[84:87]\n\t"
        "ds_read_b128 v[156:159], v166 offset:33792\n\t"
        "s_waitcnt lgkmcnt(7)\n\t"
        "v_mfma_f32_16x16x32_bf16 v[72:75], v[56:59], v[136:139], v[72:75]\n\t"
        "s_barrier\n\t"
        "s_setprio 1\n\t"
        "v_mfma_f32_16x16x32_bf16 v[88:91], v[60:63], v[136:139], v[88:91]\n\t"
        "ds_read_b128 v[136:139], v166 offset:34816\n\t"
        "s_waitcnt lgkmcnt(7)\n\t"
        "v_mfma_f32_16x16x32_bf16 v[76:79], v[56:59], v[140:143], v[76:79]\n\t"
        "v_mfma_f32_16x16x32_bf16 v[92:95], v[60:63], v[140:143], v[92:95]\n\t"
        "ds_read_b128 v[140:143], v166 offset:35840\n\t"
        "s_waitcnt lgkmcnt(7)\n\t"
        "v_mfma_f32_16x16x32_bf16 v[128:131], v[144:147], v[56:59], v[128:131]\n\t"
        "ds_read_b128 v[144:147], v166 offset:8192\n\t"
        "s_waitcnt lgkmcnt(5)\n\t"
        "v_mfma_f32_16x16x32_bf16 v[132:135], v[148:151], v[56:59], v[132:135]\n\t"
        "ds_read_b128 v[148:151], v166 offset:9216\n\t"
        "s_setprio 0\n\t"
        "s_waitcnt lgkmcnt(5)\n\t"
        "v_mfma_f32_16x16x32_bf16 v[96:99], v[56:59], v[152:155], v[96:99]\n\t"
        "v_mfma_f32_16x16x32_bf16 v[112:115], v[60:63], v[152:155], v[112:115]\n\t"
        "ds_read_b128 v[152:155], v166 offset:10240\n\t"
        "s_add_u32 s14, s15, s55\n\t"
        "s_add_u32 m0, s57, 3072\n\t"
        "s_add_u32 s59, s14, 0\n\t"
        "buffer_load_dwordx4 v162, s[24:27], s59 offen lds\n\t"
        "s_waitcnt lgkmcnt(5)\n\t"
        "v_mfma_f32_16x16x32_bf16 v[100:103], v[56:59], v[156:159], v[100:103]\n\t"
        "s_add_u32 m0, s57, 7232\n\t"
        "s_add_u32 s59, s14, 9216\n\t"
        "buffer_load_dwordx4 v162, s[24:27], s59 offen lds\n\t"
        "v_mfma_f32_16x16x32_bf16 v[116:119], v[60:63], v[156:159], v[116:119]\n\t"
        "ds_read_b128 v[156:159], v166 offset:11264\n\t"
        "s_waitcnt lgkmcnt(5)\n\t"
        "v_mfma_f32_16x16x32_bf16 v[104:107], v[56:59], v[136:139], v[104:107]\n\t"
        "s_barrier\n\t"
        "s_setprio 1\n\t"
        "v_mfma_f32_16x16x32_bf16 v[120:123], v[60:63], v[136:139], v[120:123]\n\t"
        "ds_read_b128 v[136:139], v160 offset:12288\n\t"
        "s_waitcnt vmcnt(4)\n\t"
        "ds_read_b128 v[4:7], v161 offset:1024\n\t"
        "ds_read_b128 v[12:15], v161 offset:1088\n\t"
        "s_waitcnt lgkmcnt(7)\n\t"
        "v_mfma_f32_16x16x32_bf16 v[108:111], v[56:59], v[140:143], v[108:111]\n\t"
        "v_mfma_f32_16x16x32_bf16 v[124:127], v[60:63], v[140:143], v[124:127]\n\t"
        "ds_read_b128 v[140:143], v160 offset:12304\n\t"
        "s_waitcnt lgkmcnt(7)\n\t"
        "v_mfma_f32_16x16x32_bf16 v[64:67], v[0:3], v[144:147], v[64:67]\n\t"
        "s_waitcnt lgkmcnt(2)\n\t"
        "v_mfma_f32_16x16x32_bf16 v[80:83], v[4:7], v[144:147], v[80:83]\n\t"
        "ds_read_b128 v[144:147], v166 offset:16384\n\t"
        "v_mfma_f32_16x16x32_bf16 v[68:71], v[0:3], v[148:151], v[68:71]\n\t"
        "v_mfma_f32_16x16x32_bf16 v[84:87], v[4:7], v[148:151], v[84:87]\n\t"
        "ds_read_b128 v[148:151], v166 offset:17408\n\t"
        "v_mfma_f32_16x16x32_bf16 v[72:75], v[0:3], v[152:155], v[72:75]\n\t"
        "s_bfe_u32 s15, s84, 0x40018\n\t"
        "s_lshl_b32 s15, s15, 7\n\t"
        "s_add_u32 s14, s15, s54\n\t"
        "s_add_u32 m0, s57, 0\n\t"
        "s_add_u32 s59, s14, 0\n\t"
        "buffer_load_dwordx4 v162, s[24:27], s59 offen lds\n\t"
        "v_mfma_f32_16x16x32_bf16 v[88:91], v[4:7], v[152:155], v[88:91]\n\t"
        "ds_read_b128 v[152:155], v166 offset:18432\n\t"
        "s_add_u32 m0, s57, 4160\n\t"
        "s_add_u32 s59, s14, 9216\n\t"
        "buffer_load_dwordx4 v162, s[24:27], s59 offen lds\n\t"
        "s_setprio 0\n\t"
        "v_mfma_f32_16x16x32_bf16 v[76:79], v[0:3], v[156:159], v[76:79]\n\t"
        "v_mfma_f32_16x16x32_bf16 v[92:95], v[4:7], v[156:159], v[92:95]\n\t"
        "ds_read_b128 v[156:159], v166 offset:19456\n\t"
        "v_mfma_f32_16x16x32_bf16 v[128:131], v[136:139], v[0:3], v[128:131]\n\t"
        "ds_read_b128 v[136:139], v166 offset:20480\n\t"
        "s_waitcnt lgkmcnt(5)\n\t"
        "v_mfma_f32_16x16x32_bf16 v[132:135], v[140:143], v[0:3], v[132:135]\n\t"
        "ds_read_b128 v[140:143], v166 offset:21504\n\t"
        "s_waitcnt lgkmcnt(5)\n\t"
        "v_mfma_f32_16x16x32_bf16 v[96:99], v[0:3], v[144:147], v[96:99]\n\t"
        "v_mfma_f32_16x16x32_bf16 v[112:115], v[4:7], v[144:147], v[112:115]\n\t"
        "ds_read_b128 v[144:147], v166 offset:22528\n\t"
        "s_waitcnt lgkmcnt(5)\n\t"
        "v_mfma_f32_16x16x32_bf16 v[100:103], v[0:3], v[148:151], v[100:103]\n\t"
        "v_mfma_f32_16x16x32_bf16 v[116:119], v[4:7], v[148:151], v[116:119]\n\t"
        "ds_read_b128 v[148:151], v166 offset:23552\n\t"
        "s_add_u32 s14, s15, s55\n\t"
        "s_add_u32 m0, s57, 1024\n\t"
        "s_add_u32 s59, s14, 0\n\t"
        "buffer_load_dwordx4 v162, s[24:27], s59 offen lds\n\t"
        "s_waitcnt lgkmcnt(5)\n\t"
        "v_mfma_f32_16x16x32_bf16 v[104:107], v[0:3], v[152:155], v[104:107]\n\t"
        "s_barrier\n\t"
        "s_add_u32 m0, s57, 5184\n\t"
        "s_add_u32 s59, s14, 9216\n\t"
        "buffer_load_dwordx4 v162, s[24:27], s59 offen lds\n\t"
        "s_setprio 1\n\t"
        "v_mfma_f32_16x16x32_bf16 v[120:123], v[4:7], v[152:155], v[120:123]\n\t"
        "ds_read_b128 v[152:155], v160 offset:12800\n\t"
        "s_waitcnt lgkmcnt(5)\n\t"
        "v_mfma_f32_16x16x32_bf16 v[108:111], v[0:3], v[156:159], v[108:111]\n\t"
        "s_waitcnt vmcnt(6)\n\t"
        "ds_read_b128 v[16:19], v161 offset:2048\n\t"
        "ds_read_b128 v[24:27], v161 offset:2112\n\t"
        "v_mfma_f32_16x16x32_bf16 v[124:127], v[4:7], v[156:159], v[124:127]\n\t"
        "ds_read_b128 v[156:159], v160 offset:12816\n\t"
        "s_waitcnt lgkmcnt(7)\n\t"
        "v_mfma_f32_16x16x32_bf16 v[64:67], v[8:11], v[136:139], v[64:67]\n\t"
        "v_mfma_f32_16x16x32_bf16 v[80:83], v[12:15], v[136:139], v[80:83]\n\t"
        "ds_read_b128 v[136:139], v166 offset:24576\n\t"
        "s_waitcnt lgkmcnt(7)\n\t"
        "v_mfma_f32_16x16x32_bf16 v[68:71], v[8:11], v[140:143], v[68:71]\n\t"
        "v_mfma_f32_16x16x32_bf16 v[84:87], v[12:15], v[140:143], v[84:87]\n\t"
        "ds_read_b128 v[140:143], v166 offset:25600\n\t"
        "s_waitcnt lgkmcnt(7)\n\t"
        "v_mfma_f32_16x16x32_bf16 v[72:75], v[8:11], v[144:147], v[72:75]\n\t"
        "s_setprio 0\n\t"
        "v_mfma_f32_16x16x32_bf16 v[88:91], v[12:15], v[144:147], v[88:91]\n\t"
        "ds_read_b128 v[144:147], v166 offset:26624\n\t"
        "s_waitcnt lgkmcnt(7)\n\t"
        "v_mfma_f32_16x16x32_bf16 v[76:79], v[8:11], v[148:151], v[76:79]\n\t"
        "s_waitcnt lgkmcnt(4)\n\t"
        "s_bfe_u32 s15, s84, 0x4001c\n\t"
        "s_lshl_b32 s15, s15, 7\n\t"
        "s_add_u32 s14, s15, s54\n\t"
        "s_add_u32 m0, s57, 2048\n\t"
        "s_add_u32 s59, s14, 0\n\t"
        "buffer_load_dwordx4 v162, s[24:27], s59 offen lds\n\t"
        "v_mfma_f32_16x16x32_bf16 v[92:95], v[12:15], v[148:151], v[92:95]\n\t"
        "ds_read_b128 v[148:151], v166 offset:27648\n\t"
        "s_add_u32 m0, s57, 6208\n\t"
        "s_add_u32 s59, s14, 9216\n\t"
        "buffer_load_dwordx4 v162, s[24:27], s59 offen lds\n\t"
        "v_mfma_f32_16x16x32_bf16 v[128:131], v[152:155], v[8:11], v[128:131]\n\t"
        "ds_read_b128 v[152:155], v166 offset:28672\n\t"
        "s_waitcnt lgkmcnt(5)\n\t"
        "v_mfma_f32_16x16x32_bf16 v[132:135], v[156:159], v[8:11], v[132:135]\n\t"
        "ds_read_b128 v[156:159], v166 offset:29696\n\t"
        "s_waitcnt lgkmcnt(5)\n\t"
        "v_mfma_f32_16x16x32_bf16 v[96:99], v[8:11], v[136:139], v[96:99]\n\t"
        "v_mfma_f32_16x16x32_bf16 v[112:115], v[12:15], v[136:139], v[112:115]\n\t"
        "ds_read_b128 v[136:139], v166 offset:30720\n\t"
        "s_waitcnt lgkmcnt(5)\n\t"
        "v_mfma_f32_16x16x32_bf16 v[100:103], v[8:11], v[140:143], v[100:103]\n\t"
        "s_barrier\n\t"
        "s_setprio 1\n\t"
        "v_mfma_f32_16x16x32_bf16 v[116:119], v[12:15], v[140:143], v[116:119]\n\t"
        "ds_read_b128 v[140:143], v166 offset:31744\n\t"
        "s_waitcnt lgkmcnt(5)\n\t"
        "v_mfma_f32_16x16x32_bf16 v[104:107], v[8:11], v[144:147], v[104:107]\n\t"
        "v_mfma_f32_16x16x32_bf16 v[120:123], v[12:15], v[144:147], v[120:123]\n\t"
        "ds_read_b128 v[144:147], v160 offset:0\n\t"
        "s_waitcnt vmcnt(6)\n\t"
        "ds_read_b128 v[20:23], v161 offset:3072\n\t"
        "ds_read_b128 v[28:31], v161 offset:3136\n\t"
        "s_waitcnt lgkmcnt(7)\n\t"
        "v_mfma_f32_16x16x32_bf16 v[108:111], v[8:11], v[148:151], v[108:111]\n\t"
        "v_mfma_f32_16x16x32_bf16 v[124:127], v[12:15], v[148:151], v[124:127]\n\t"
        "ds_read_b128 v[148:151], v160 offset:16\n\t"
        "s_waitcnt lgkmcnt(7)\n\t"
        "v_mfma_f32_16x16x32_bf16 v[64:67], v[16:19], v[152:155], v[64:67]\n\t"
        "s_setprio 0\n\t"
        "s_waitcnt lgkmcnt(2)\n\t"
        "v_mfma_f32_16x16x32_bf16 v[80:83], v[20:23], v[152:155], v[80:83]\n\t"
        "ds_read_b128 v[152:155], v166 offset:4096\n\t"
        "s_waitcnt lgkmcnt(2)\n\t"
        "s_add_u32 s14, s15, s55\n\t"
        "s_add_u32 m0, s57, 3072\n\t"
        "s_add_u32 s59, s14, 0\n\t"
        "buffer_load_dwordx4 v162, s[24:27], s59 offen lds\n\t"
        "v_mfma_f32_16x16x32_bf16 v[68:71], v[16:19], v[156:159], v[68:71]\n\t"
        "s_add_u32 m0, s57, 7232\n\t"
        "s_add_u32 s59, s14, 9216\n\t"
        "buffer_load_dwordx4 v162, s[24:27], s59 offen lds\n\t"
        "v_mfma_f32_16x16x32_bf16 v[84:87], v[20:23], v[156:159], v[84:87]\n\t"
        "ds_read_b128 v[156:159], v166 offset:5120\n\t"
        "v_mfma_f32_16x16x32_bf16 v[72:75], v[16:19], v[136:139], v[72:75]\n\t"
        "v_mfma_f32_16x16x32_bf16 v[88:91], v[20:23], v[136:139], v[88:91]\n\t"
        "ds_read_b128 v[136:139], v166 offset:6144\n\t"
        "v_mfma_f32_16x16x32_bf16 v[76:79], v[16:19], v[140:143], v[76:79]\n\t"
        "s_barrier\n\t"
        "s_setprio 1\n\t"
        "v_mfma_f32_16x16x32_bf16 v[92:95], v[20:23], v[140:143], v[92:95]\n\t"
        "ds_read_b128 v[140:143], v166 offset:7168\n\t"
        "v_mfma_f32_16x16x32_bf16 v[128:131], v[144:147], v[16:19], v[128:131]\n\t"
        "ds_read_b128 v[144:147], v160 offset:512\n\t"
        "s_waitcnt vmcnt(6)\n\t"
        "ds_read_b128 v[32:35], v161 offset:0\n\t"
        "ds_read_b128 v[40:43], v161 offset:64\n\t"
        "s_waitcnt lgkmcnt(7)\n\t"
        "v_mfma_f32_16x16x32_bf16 v[132:135], v[148:151], v[16:19], v[132:135]\n\t"
        "ds_read_b128 v[148:151], v160 offset:528\n\t"
        "s_waitcnt lgkmcnt(7)\n\t"
        "v_mfma_f32_16x16x32_bf16 v[64:67], v[24:27], v[152:155], v[64:67]\n\t"
        "v_mfma_f32_16x16x32_bf16 v[80:83], v[28:31], v[152:155], v[80:83]\n\t"
        "ds_read_b128 v[152:155], v166 offset:8192\n\t"
        "s_setprio 0\n\t"
        "s_waitcnt lgkmcnt(7)\n\t"
        "v_mfma_f32_16x16x32_bf16 v[68:71], v[24:27], v[156:159], v[68:71]\n\t"
        "s_waitcnt lgkmcnt(2)\n\t"
        "s_bfe_u32 s15, s85, 0x40000\n\t"
        "s_lshl_b32 s15, s15, 7\n\t"
        "s_add_u32 s14, s15, s54\n\t"
        "s_add_u32 m0, s57, 0\n\t"
        "s_add_u32 s59, s14, 0\n\t"
        "buffer_load_dwordx4 v162, s[24:27], s59 offen lds\n\t"
        "v_mfma_f32_16x16x32_bf16 v[84:87], v[28:31], v[156:159], v[84:87]\n\t"
        "ds_read_b128 v[156:159], v166 offset:9216\n\t"
        "s_add_u32 m0, s57, 4160\n\t"
        "s_add_u32 s59, s14, 9216\n\t"
        "buffer_load_dwordx4 v162, s[24:27], s59 offen lds\n\t"
        "v_mfma_f32_16x16x32_bf16 v[72:75], v[24:27], v[136:139], v[72:75]\n\t"
        "v_mfma_f32_16x16x32_bf16 v[88:91], v[28:31], v[136:139], v[88:91]\n\t"
        "ds_read_b128 v[136:139], v166 offset:10240\n\t"
        "v_mfma_f32_16x16x32_bf16 v[76:79], v[24:27], v[140:143], v[76:79]\n\t"
        "s_barrier\n\t"
        "s_setprio 1\n\t"
        "v_mfma_f32_16x16x32_bf16 v[92:95], v[28:31], v[140:143], v[92:95]\n\t"
        "ds_read_b128 v[140:143], v166 offset:11264\n\t"
        "s_waitcnt vmcnt(6)\n\t"
        "ds_read_b128 v[36:39], v161 offset:1024\n\t"
        "ds_read_b128 v[44:47], v161 offset:1088\n\t"
        "v_mfma_f32_16x16x32_bf16 v[128:131], v[144:147], v[24:27], v[128:131]\n\t"
        "ds_read_b128 v[144:147], v160 offset:32768\n\t"
        "s_waitcnt lgkmcnt(7)\n\t"
        "v_mfma_f32_16x16x32_bf16 v[132:135], v[148:151], v[24:27], v[132:135]\n\t"
        "ds_read_b128 v[148:151], v160 offset:32784\n\t"
        "s_waitcnt lgkmcnt(7)\n\t"
        "v_mfma_f32_16x16x32_bf16 v[64:67], v[32:35], v[152:155], v[64:67]\n\t"
        "s_waitcnt lgkmcnt(3)\n\t"
        "v_mfma_f32_16x16x32_bf16 v[80:83], v[36:39], v[152:155], v[80:83]\n\t"
        "ds_read_b128 v[152:155], v166 offset:12288\n\t"
        "s_waitcnt lgkmcnt(3)\n\t"
        "s_add_u32 s14, s15, s55\n\t"
        "s_add_u32 m0, s57, 1024\n\t"
        "s_add_u32 s59, s14, 0\n\t"
        "buffer_load_dwordx4 v162, s[24:27], s59 offen lds\n\t"
        "v_mfma_f32_16x16x32_bf16 v[68:71], v[32:35], v[156:159], v[68:71]\n\t"
        "s_add_u32 m0, s57, 5184\n\t"
        "s_add_u32 s59, s14, 9216\n\t"
        "buffer_load_dwordx4 v162, s[24:27], s59 offen lds\n\t"
        "v_mfma_f32_16x16x32_bf16 v[84:87], v[36:39], v[156:159], v[84:87]\n\t"
        "ds_read_b128 v[156:159], v166 offset:13312\n\t"
        "v_mfma_f32_16x16x32_bf16 v[72:75], v[32:35], v[136:139], v[72:75]\n\t"
        "v_mfma_f32_16x16x32_bf16 v[88:91], v[36:39], v[136:139], v[88:91]\n\t"
        "ds_read_b128 v[136:139], v166 offset:14336\n\t"
        "s_setprio 0\n\t"
        "v_mfma_f32_16x16x32_bf16 v[76:79], v[32:35], v[140:143], v[76:79]\n\t"
        "v_mfma_f32_16x16x32_bf16 v[92:95], v[36:39], v[140:143], v[92:95]\n\t"
        "ds_read_b128 v[140:143], v166 offset:15360\n\t"
        "s_waitcnt lgkmcnt(5)\n\t"
        "v_mfma_f32_16x16x32_bf16 v[128:131], v[144:147], v[32:35], v[128:131]\n\t"
        "ds_read_b128 v[144:147], v160 offset:33280\n\t"
        "s_waitcnt vmcnt(6)\n\t"
        "ds_read_b128 v[48:51], v161 offset:2048\n\t"
        "ds_read_b128 v[56:59], v161 offset:2112\n\t"
        "s_waitcnt lgkmcnt(7)\n\t"
        "v_mfma_f32_16x16x32_bf16 v[132:135], v[148:151], v[32:35], v[132:135]\n\t"
        "ds_read_b128 v[148:151], v160 offset:33296\n\t"
        "s_waitcnt lgkmcnt(7)\n\t"
        "v_mfma_f32_16x16x32_bf16 v[64:67], v[40:43], v[152:155], v[64:67]\n\t"
        "v_mfma_f32_16x16x32_bf16 v[80:83], v[44:47], v[152:155], v[80:83]\n\t"
        "ds_read_b128 v[152:155], v166 offset:16384\n\t"
        "s_waitcnt lgkmcnt(7)\n\t"
        "v_mfma_f32_16x16x32_bf16 v[68:71], v[40:43], v[156:159], v[68:71]\n\t"
        "v_mfma_f32_16x16x32_bf16 v[84:87], v[44:47], v[156:159], v[84:87]\n\t"
        "ds_read_b128 v[156:159], v166 offset:17408\n\t"
        "s_waitcnt lgkmcnt(7)\n\t"
        "v_mfma_f32_16x16x32_bf16 v[72:75], v[40:43], v[136:139], v[72:75]\n\t"
        "s_barrier\n\t"
        "s_setprio 1\n\t"
        "v_mfma_f32_16x16x32_bf16 v[88:91], v[44:47], v[136:139], v[88:91]\n\t"
        "ds_read_b128 v[136:139], v166 offset:18432\n\t"
        "s_waitcnt lgkmcnt(7)\n\t"
        "v_mfma_f32_16x16x32_bf16 v[76:79], v[40:43], v[140:143], v[76:79]\n\t"
        "v_mfma_f32_16x16x32_bf16 v[92:95], v[44:47], v[140:143], v[92:95]\n\t"
        "ds_read_b128 v[140:143], v166 offset:19456\n\t"
        "s_waitcnt vmcnt(4)\n\t"
        "ds_read_b128 v[52:55], v161 offset:3072\n\t"
        "ds_read_b128 v[60:63], v161 offset:3136\n\t"
        "s_waitcnt lgkmcnt(9)\n\t"
        "v_mfma_f32_16x16x32_bf16 v[128:131], v[144:147], v[40:43], v[128:131]\n\t"
        "ds_read_b128 v[144:147], v160 offset:20480\n\t"
        "s_waitcnt lgkmcnt(7)\n\t"
        "v_mfma_f32_16x16x32_bf16 v[132:135], v[148:151], v[40:43], v[132:135]\n\t"
        "ds_read_b128 v[148:151], v160 offset:20496\n\t"
        "s_waitcnt lgkmcnt(7)\n\t"
        "v_mfma_f32_16x16x32_bf16 v[64:67], v[48:51], v[152:155], v[64:67]\n\t"
        "s_setprio 0\n\t"
        "s_waitcnt lgkmcnt(3)\n\t"
        "v_mfma_f32_16x16x32_bf16 v[80:83], v[52:55], v[152:155], v[80:83]\n\t"
        "ds_read_b128 v[152:155], v166 offset:24576\n\t"
        "v_mfma_f32_16x16x32_bf16 v[68:71], v[48:51], v[156:159], v[68:71]\n\t"
        "v_mfma_f32_16x16x32_bf16 v[84:87], v[52:55], v[156:159], v[84:87]\n\t"
        "ds_read_b128 v[156:159], v166 offset:25600\n\t"
        "v_mfma_f32_16x16x32_bf16 v[72:75], v[48:51], v[136:139], v[72:75]\n\t"
        "v_mfma_f32_16x16x32_bf16 v[88:91], v[52:55], v[136:139], v[88:91]\n\t"
        "ds_read_b128 v[136:139], v166 offset:26624\n\t"
        "v_mfma_f32_16x16x32_bf16 v[76:79], v[48:51], v[140:143], v[76:79]\n\t"
        "s_barrier\n\t"
        "s_setprio 1\n\t"
        "v_mfma_f32_16x16x32_bf16 v[92:95], v[52:55], v[140:143], v[92:95]\n\t"
        "ds_read_b128 v[140:143], v166 offset:27648\n\t"
        "s_waitcnt lgkmcnt(5)\n\t"
        "v_mfma_f32_16x16x32_bf16 v[128:131], v[144:147], v[48:51], v[128:131]\n\t"
        "ds_read_b128 v[144:147], v160 offset:20992\n\t"
        "s_waitcnt vmcnt(2)\n\t"
        "ds_read_b128 v[0:3], v161 offset:0\n\t"
        "ds_read_b128 v[8:11], v161 offset:64\n\t"
        "s_waitcnt lgkmcnt(7)\n\t"
        "v_mfma_f32_16x16x32_bf16 v[132:135], v[148:151], v[48:51], v[132:135]\n\t"
        "ds_read_b128 v[148:151], v160 offset:21008\n\t"
        "s_waitcnt lgkmcnt(7)\n\t"
        "v_mfma_f32_16x16x32_bf16 v[64:67], v[56:59], v[152:155], v[64:67]\n\t"
        "v_mfma_f32_16x16x32_bf16 v[80:83], v[60:63], v[152:155], v[80:83]\n\t"
        "ds_read_b128 v[152:155], v166 offset:28672\n\t"
        "s_setprio 0\n\t"
        "s_waitcnt lgkmcnt(7)\n\t"
        "v_mfma_f32_16x16x32_bf16 v[68:71], v[56:59], v[156:159], v[68:71]\n\t"
        "v_mfma_f32_16x16x32_bf16 v[84:87], v[60:63], v[156:159], v[84:87]\n\t"
        "ds_read_b128 v[156:159], v166 offset:29696\n\t"
        "s_waitcnt lgkmcnt(7)\n\t"
        "v_mfma_f32_16x16x32_bf16 v[72:75], v[56:59], v[136:139], v[72:75]\n\t"
        "v_mfma_f32_16x16x32_bf16 v[88:91], v[60:63], v[136:139], v[88:91]\n\t"
        "ds_read_b128 v[136:139], v166 offset:30720\n\t"
        "s_waitcnt lgkmcnt(7)\n\t"
        "v_mfma_f32_16x16x32_bf16 v[76:79], v[56:59], v[140:143], v[76:79]\n\t"
        "s_barrier\n\t"
        "s_setprio 1\n\t"
        "v_mfma_f32_16x16x32_bf16 v[92:95], v[60:63], v[140:143], v[92:95]\n\t"
        "ds_read_b128 v[140:143], v166 offset:31744\n\t"
        "s_waitcnt vmcnt(0)\n\t"
        "ds_read_b128 v[4:7], v161 offset:1024\n\t"
        "ds_read_b128 v[12:15], v161 offset:1088\n\t"
        "s_waitcnt lgkmcnt(9)\n\t"
        "v_mfma_f32_16x16x32_bf16 v[128:131], v[144:147], v[56:59], v[128:131]\n\t"
        "ds_read_b128 v[144:147], v160 offset:36864\n\t"
        "s_waitcnt lgkmcnt(7)\n\t"
        "v_mfma_f32_16x16x32_bf16 v[132:135], v[148:151], v[56:59], v[132:135]\n\t"
        "ds_read_b128 v[148:151], v160 offset:36880\n\t"
        "s_waitcnt lgkmcnt(7)\n\t"
        "v_mfma_f32_16x16x32_bf16 v[64:67], v[0:3], v[152:155], v[64:67]\n\t"
        "s_waitcnt lgkmcnt(3)\n\t"
        "v_mfma_f32_16x16x32_bf16 v[80:83], v[4:7], v[152:155], v[80:83]\n\t"
        "ds_read_b128 v[152:155], v166 offset:0\n\t"
        "v_mfma_f32_16x16x32_bf16 v[68:71], v[0:3], v[156:159], v[68:71]\n\t"
        "v_mfma_f32_16x16x32_bf16 v[84:87], v[4:7], v[156:159], v[84:87]\n\t"
        "ds_read_b128 v[156:159], v166 offset:1024\n\t"
        "v_mfma_f32_16x16x32_bf16 v[72:75], v[0:3], v[136:139], v[72:75]\n\t"
        "v_mfma_f32_16x16x32_bf16 v[88:91], v[4:7], v[136:139], v[88:91]\n\t"
        "ds_read_b128 v[136:139], v166 offset:2048\n\t"
        "s_setprio 0\n\t"
        "v_mfma_f32_16x16x32_bf16 v[76:79], v[0:3], v[140:143], v[76:79]\n\t"
        "v_mfma_f32_16x16x32_bf16 v[92:95], v[4:7], v[140:143], v[92:95]\n\t"
        "ds_read_b128 v[140:143], v166 offset:3072\n\t"
        "s_waitcnt lgkmcnt(5)\n\t"
        "v_mfma_f32_16x16x32_bf16 v[128:131], v[144:147], v[0:3], v[128:131]\n\t"
        "ds_read_b128 v[144:147], v160 offset:37376\n\t"
        "s_waitcnt lgkmcnt(5)\n\t"
        "v_mfma_f32_16x16x32_bf16 v[132:135], v[148:151], v[0:3], v[132:135]\n\t"
        "ds_read_b128 v[148:151], v160 offset:37392\n\t"
        "s_waitcnt lgkmcnt(5)\n\t"
        "v_mfma_f32_16x16x32_bf16 v[64:67], v[8:11], v[152:155], v[64:67]\n\t"
        "v_mfma_f32_16x16x32_bf16 v[80:83], v[12:15], v[152:155], v[80:83]\n\t"
        "ds_read_b128 v[152:155], v166 offset:4096\n\t"
        "s_waitcnt lgkmcnt(5)\n\t"
        "v_mfma_f32_16x16x32_bf16 v[68:71], v[8:11], v[156:159], v[68:71]\n\t"
        "v_mfma_f32_16x16x32_bf16 v[84:87], v[12:15], v[156:159], v[84:87]\n\t"
        "ds_read_b128 v[156:159], v166 offset:5120\n\t"
        "s_waitcnt lgkmcnt(5)\n\t"
        "v_mfma_f32_16x16x32_bf16 v[72:75], v[8:11], v[136:139], v[72:75]\n\t"
        "s_barrier\n\t"
        "s_setprio 1\n\t"
        "v_mfma_f32_16x16x32_bf16 v[88:91], v[12:15], v[136:139], v[88:91]\n\t"
        "ds_read_b128 v[136:139], v166 offset:6144\n\t"
        "s_waitcnt lgkmcnt(5)\n\t"
        "v_mfma_f32_16x16x32_bf16 v[76:79], v[8:11], v[140:143], v[76:79]\n\t"
        "v_mfma_f32_16x16x32_bf16 v[92:95], v[12:15], v[140:143], v[92:95]\n\t"
        "ds_read_b128 v[140:143], v166 offset:7168\n\t"
        "s_waitcnt lgkmcnt(5)\n\t"
        "v_mfma_f32_16x16x32_bf16 v[128:131], v[144:147], v[8:11], v[128:131]\n\t"
        "ds_read_b128 v[144:147], v166 offset:8192\n\t"
        "s_waitcnt lgkmcnt(5)\n\t"
        "v_mfma_f32_16x16x32_bf16 v[132:135], v[148:151], v[8:11], v[132:135]\n\t"
        "ds_read_b128 v[148:151], v166 offset:9216\n\t"
        "v_lshrrev_b32 v164, 8, v166\n\t"
        "v_lshlrev_b32 v160, 4, v164\n\t"
        "v_bfe_u32 v165, v166, 4, 4\n\t"
        "v_mul_u32_u24 v165, 9216, v165\n\t"
        "s_lshl_b32 s53, s53, 1\n\t"
        "s_lshl_b32 s52, s52, 1\n\t"
        "v_add3_u32 v160, v160, v165, s53\n\t"
        "v_bfe_u32 v165, v166, 4, 4\n\t"
        "v_lshlrev_b32 v164, 9, v164\n\t"
        "v_lshl_add_u32 v165, v165, 3, v164\n\t"
        "v_add_u32 v162, s57, v165\n\t"
        "v_lshrrev_b32 v165, 7, v166\n\t"
        "v_mul_u32_u24 v165, 9216, v165\n\t"
        "v_and_b32 v164, 0x70, v166\n\t"
        "v_add3_u32 v163, v165, v164, s52\n\t"
        "s_waitcnt lgkmcnt(5)\n\t"
        "v_mfma_f32_16x16x32_bf16 v[96:99], v[16:19], v[152:155], v[96:99]\n\t"
        "v_cvt_pk_bf16_f32 v128, v128, v129\n\t"
        "v_cvt_pk_bf16_f32 v129, v130, v131\n\t"
        "v_cvt_pk_bf16_f32 v130, v132, v133\n\t"
        "s_setprio 0\n\t"
        "v_mfma_f32_16x16x32_bf16 v[112:115], v[20:23], v[152:155], v[112:115]\n\t"
        "ds_read_b128 v[152:155], v166 offset:10240\n\t"
        "v_cvt_pk_bf16_f32 v131, v134, v135\n\t"
        "buffer_store_dwordx4 v[128:131], v160, s[36:39], s90 offen\n\t"
        "v_cvt_pk_bf16_f32 v132, v64, v68\n\t"
        "s_waitcnt lgkmcnt(5)\n\t"
        "v_mfma_f32_16x16x32_bf16 v[100:103], v[16:19], v[156:159], v[100:103]\n\t"
        "v_cvt_pk_bf16_f32 v133, v72, v76\n\t"
        "ds_write_b64 v162, v[132:133] offset:0\n\t"
        "v_cvt_pk_bf16_f32 v134, v65, v69\n\t"
        "v_mfma_f32_16x16x32_bf16 v[116:119], v[20:23], v[156:159], v[116:119]\n\t"
        "ds_read_b128 v[156:159], v166 offset:11264\n\t"
        "v_cvt_pk_bf16_f32 v135, v73, v77\n\t"
        "ds_write_b64 v162, v[134:135] offset:128\n\t"
        "v_cvt_pk_bf16_f32 v132, v66, v70\n\t"
        "s_waitcnt lgkmcnt(7)\n\t"
        "v_mfma_f32_16x16x32_bf16 v[104:107], v[16:19], v[136:139], v[104:107]\n\t"
        "v_cvt_pk_bf16_f32 v133, v74, v78\n\t"
        "ds_write_b64 v162, v[132:133] offset:256\n\t"
        "v_cvt_pk_bf16_f32 v134, v67, v71\n\t"
        "v_mfma_f32_16x16x32_bf16 v[120:123], v[20:23], v[136:139], v[120:123]\n\t"
        "ds_read_b128 v[136:139], v166 offset:12288\n\t"
        "v_cvt_pk_bf16_f32 v135, v75, v79\n\t"
        "ds_write_b64 v162, v[134:135] offset:384\n\t"
        "v_cvt_pk_bf16_f32 v132, v80, v84\n\t"
        "s_waitcnt lgkmcnt(9)\n\t"
        "v_mfma_f32_16x16x32_bf16 v[108:111], v[16:19], v[140:143], v[108:111]\n\t"
        "s_barrier\n\t"
        "v_cvt_pk_bf16_f32 v133, v88, v92\n\t"
        "ds_write_b64 v162, v[132:133] offset:2048\n\t"
        "v_cvt_pk_bf16_f32 v134, v81, v85\n\t"
        "s_setprio 1\n\t"
        "v_mfma_f32_16x16x32_bf16 v[124:127], v[20:23], v[140:143], v[124:127]\n\t"
        "ds_read_b128 v[140:143], v166 offset:13312\n\t"
        "v_cvt_pk_bf16_f32 v135, v89, v93\n\t"
        "ds_write_b64 v162, v[134:135] offset:2176\n\t"
        "v_cvt_pk_bf16_f32 v132, v82, v86\n\t"
        "s_waitcnt lgkmcnt(11)\n\t"
        "v_mfma_f32_16x16x32_bf16 v[96:99], v[24:27], v[144:147], v[96:99]\n\t"
        "v_cvt_pk_bf16_f32 v133, v90, v94\n\t"
        "ds_write_b64 v162, v[132:133] offset:2304\n\t"
        "v_cvt_pk_bf16_f32 v134, v83, v87\n\t"
        "v_mfma_f32_16x16x32_bf16 v[112:115], v[28:31], v[144:147], v[112:115]\n\t"
        "ds_read_b128 v[144:147], v166 offset:14336\n\t"
        "v_cvt_pk_bf16_f32 v135, v91, v95\n\t"
        "ds_write_b64 v162, v[134:135] offset:2432\n\t"
        "s_waitcnt lgkmcnt(13)\n\t"
        "v_mfma_f32_16x16x32_bf16 v[100:103], v[24:27], v[148:151], v[100:103]\n\t"
        "ds_read_b128 v[64:67], v167 offset:0\n\t"
        "ds_read_b128 v[68:71], v167 offset:1024\n\t"
        "ds_read_b128 v[72:75], v167 offset:2048\n\t"
        "ds_read_b128 v[76:79], v167 offset:3072\n\t"
        "s_setprio 0\n\t"
        "v_mfma_f32_16x16x32_bf16 v[116:119], v[28:31], v[148:151], v[116:119]\n\t"
        "ds_read_b128 v[148:151], v166 offset:15360\n\t"
        "s_waitcnt lgkmcnt(15)\n\t"
        "v_mfma_f32_16x16x32_bf16 v[104:107], v[24:27], v[152:155], v[104:107]\n\t"
        "v_mfma_f32_16x16x32_bf16 v[120:123], v[28:31], v[152:155], v[120:123]\n\t"
        "ds_read_b128 v[152:155], v166 offset:16384\n\t"
        "v_mfma_f32_16x16x32_bf16 v[108:111], v[24:27], v[156:159], v[108:111]\n\t"
        "s_barrier\n\t"
        "s_waitcnt lgkmcnt(5)\n\t"
        "s_add_u32 s88, s90, 0\n\t"
        "buffer_store_dwordx4 v[64:67], v163, s[36:39], s88 offen nt\n\t"
        "s_setprio 1\n\t"
        "v_mfma_f32_16x16x32_bf16 v[124:127], v[28:31], v[156:159], v[124:127]\n\t"
        "ds_read_b128 v[156:159], v166 offset:17408\n\t"
        "v_mfma_f32_16x16x32_bf16 v[96:99], v[32:35], v[136:139], v[96:99]\n\t"
        "ds_read_b128 v[136:139], v166 offset:18432\n\t"
        "v_mfma_f32_16x16x32_bf16 v[100:103], v[32:35], v[140:143], v[100:103]\n\t"
        "ds_read_b128 v[140:143], v166 offset:19456\n\t"
        "v_mfma_f32_16x16x32_bf16 v[104:107], v[32:35], v[144:147], v[104:107]\n\t"
        "ds_read_b128 v[144:147], v166 offset:32768\n\t"
        "s_waitcnt lgkmcnt(8)\n\t"
        "s_add_u32 s88, s90, 73728\n\t"
        "buffer_store_dwordx4 v[68:71], v163, s[36:39], s88 offen nt\n\t"
        "s_setprio 0\n\t"
        "s_waitcnt lgkmcnt(5)\n\t"
        "v_mfma_f32_16x16x32_bf16 v[108:111], v[32:35], v[148:151], v[108:111]\n\t"
        "ds_read_b128 v[148:151], v166 offset:33792\n\t"
        "s_waitcnt lgkmcnt(5)\n\t"
        "v_mfma_f32_16x16x32_bf16 v[96:99], v[40:43], v[152:155], v[96:99]\n\t"
        "ds_read_b128 v[152:155], v166 offset:34816\n\t"
        "s_waitcnt lgkmcnt(5)\n\t"
        "v_mfma_f32_16x16x32_bf16 v[100:103], v[40:43], v[156:159], v[100:103]\n\t"
        "ds_read_b128 v[156:159], v166 offset:35840\n\t"
        "s_waitcnt lgkmcnt(5)\n\t"
        "v_mfma_f32_16x16x32_bf16 v[104:107], v[40:43], v[136:139], v[104:107]\n\t"
        "ds_read_b128 v[136:139], v166 offset:20480\n\t"
        "s_barrier\n\t"
        "s_add_u32 s88, s91, 0\n\t"
        "buffer_store_dwordx4 v[72:75], v163, s[36:39], s88 offen nt\n\t"
        "s_waitcnt lgkmcnt(5)\n\t"
        "v_mfma_f32_16x16x32_bf16 v[108:111], v[40:43], v[140:143], v[108:111]\n\t"
        "ds_read_b128 v[140:143], v166 offset:21504\n\t"
        "s_waitcnt lgkmcnt(5)\n\t"
        "v_mfma_f32_16x16x32_bf16 v[96:99], v[48:51], v[144:147], v[96:99]\n\t"
        "ds_read_b128 v[144:147], v166 offset:22528\n\t"
        "s_waitcnt lgkmcnt(5)\n\t"
        "v_mfma_f32_16x16x32_bf16 v[100:103], v[48:51], v[148:151], v[100:103]\n\t"
        "ds_read_b128 v[148:151], v166 offset:23552\n\t"
        "s_waitcnt lgkmcnt(5)\n\t"
        "v_mfma_f32_16x16x32_bf16 v[104:107], v[48:51], v[152:155], v[104:107]\n\t"
        "ds_read_b128 v[152:155], v166 offset:24576\n\t"
        "s_add_u32 s88, s91, 73728\n\t"
        "buffer_store_dwordx4 v[76:79], v163, s[36:39], s88 offen nt\n\t"
        "s_waitcnt lgkmcnt(5)\n\t"
        "v_mfma_f32_16x16x32_bf16 v[108:111], v[48:51], v[156:159], v[108:111]\n\t"
        "ds_read_b128 v[156:159], v166 offset:25600\n\t"
        "s_waitcnt lgkmcnt(5)\n\t"
        "v_mfma_f32_16x16x32_bf16 v[96:99], v[56:59], v[136:139], v[96:99]\n\t"
        "ds_read_b128 v[136:139], v166 offset:26624\n\t"
        "s_waitcnt lgkmcnt(5)\n\t"
        "v_mfma_f32_16x16x32_bf16 v[100:103], v[56:59], v[140:143], v[100:103]\n\t"
        "ds_read_b128 v[140:143], v166 offset:27648\n\t"
        "s_waitcnt lgkmcnt(5)\n\t"
        "v_mfma_f32_16x16x32_bf16 v[104:107], v[56:59], v[144:147], v[104:107]\n\t"
        "ds_read_b128 v[144:147], v166 offset:0\n\t"
        "s_waitcnt lgkmcnt(5)\n\t"
        "v_mfma_f32_16x16x32_bf16 v[108:111], v[56:59], v[148:151], v[108:111]\n\t"
        "ds_read_b128 v[148:151], v166 offset:1024\n\t"
        "s_waitcnt lgkmcnt(5)\n\t"
        "v_mfma_f32_16x16x32_bf16 v[96:99], v[0:3], v[152:155], v[96:99]\n\t"
        "ds_read_b128 v[152:155], v166 offset:2048\n\t"
        "s_waitcnt lgkmcnt(5)\n\t"
        "v_mfma_f32_16x16x32_bf16 v[100:103], v[0:3], v[156:159], v[100:103]\n\t"
        "ds_read_b128 v[156:159], v166 offset:3072\n\t"
        "s_waitcnt lgkmcnt(5)\n\t"
        "v_mfma_f32_16x16x32_bf16 v[104:107], v[0:3], v[136:139], v[104:107]\n\t"
        "ds_read_b128 v[136:139], v166 offset:12288\n\t"
        "s_waitcnt lgkmcnt(5)\n\t"
        "v_mfma_f32_16x16x32_bf16 v[108:111], v[0:3], v[140:143], v[108:111]\n\t"
        "ds_read_b128 v[140:143], v166 offset:13312\n\t"
        "s_waitcnt lgkmcnt(5)\n\t"
        "v_mfma_f32_16x16x32_bf16 v[96:99], v[8:11], v[144:147], v[96:99]\n\t"
        "ds_read_b128 v[144:147], v166 offset:14336\n\t"
        "s_waitcnt lgkmcnt(5)\n\t"
        "v_mfma_f32_16x16x32_bf16 v[100:103], v[8:11], v[148:151], v[100:103]\n\t"
        "ds_read_b128 v[148:151], v166 offset:15360\n\t"
        "s_waitcnt lgkmcnt(5)\n\t"
        "v_mfma_f32_16x16x32_bf16 v[104:107], v[8:11], v[152:155], v[104:107]\n\t"
        "ds_read_b128 v[152:155], v166 offset:16384\n\t"
        "s_waitcnt lgkmcnt(5)\n\t"
        "v_mfma_f32_16x16x32_bf16 v[108:111], v[8:11], v[156:159], v[108:111]\n\t"
        "ds_read_b128 v[156:159], v166 offset:17408\n\t"
        "s_waitcnt lgkmcnt(5)\n\t"
        "v_mfma_f32_16x16x32_bf16 v[112:115], v[36:39], v[136:139], v[112:115]\n\t"
        "ds_read_b128 v[136:139], v166 offset:18432\n\t"
        "s_waitcnt lgkmcnt(5)\n\t"
        "v_mfma_f32_16x16x32_bf16 v[116:119], v[36:39], v[140:143], v[116:119]\n\t"
        "ds_read_b128 v[140:143], v166 offset:19456\n\t"
        "v_cvt_pk_bf16_f32 v132, v96, v100\n\t"
        "v_cvt_pk_bf16_f32 v133, v104, v108\n\t"
        "ds_write_b64 v162, v[132:133] offset:4096\n\t"
        "s_waitcnt lgkmcnt(6)\n\t"
        "v_mfma_f32_16x16x32_bf16 v[120:123], v[36:39], v[144:147], v[120:123]\n\t"
        "ds_read_b128 v[144:147], v166 offset:32768\n\t"
        "v_cvt_pk_bf16_f32 v134, v97, v101\n\t"
        "v_cvt_pk_bf16_f32 v135, v105, v109\n\t"
        "s_waitcnt lgkmcnt(6)\n\t"
        "v_mfma_f32_16x16x32_bf16 v[124:127], v[36:39], v[148:151], v[124:127]\n\t"
        "ds_read_b128 v[148:151], v166 offset:33792\n\t"
        "ds_write_b64 v162, v[134:135] offset:4224\n\t"
        "v_cvt_pk_bf16_f32 v132, v98, v102\n\t"
        "v_cvt_pk_bf16_f32 v133, v106, v110\n\t"
        "s_waitcnt lgkmcnt(7)\n\t"
        "v_mfma_f32_16x16x32_bf16 v[112:115], v[44:47], v[152:155], v[112:115]\n\t"
        "ds_read_b128 v[152:155], v166 offset:34816\n\t"
        "ds_write_b64 v162, v[132:133] offset:4352\n\t"
        "v_cvt_pk_bf16_f32 v134, v99, v103\n\t"
        "s_waitcnt lgkmcnt(8)\n\t"
        "v_mfma_f32_16x16x32_bf16 v[116:119], v[44:47], v[156:159], v[116:119]\n\t"
        "ds_read_b128 v[156:159], v166 offset:35840\n\t"
        "v_cvt_pk_bf16_f32 v135, v107, v111\n\t"
        "ds_write_b64 v162, v[134:135] offset:4480\n\t"
        "s_waitcnt lgkmcnt(9)\n\t"
        "v_mfma_f32_16x16x32_bf16 v[120:123], v[44:47], v[136:139], v[120:123]\n\t"
        "ds_read_b128 v[136:139], v166 offset:20480\n\t"
        "s_waitcnt lgkmcnt(9)\n\t"
        "v_mfma_f32_16x16x32_bf16 v[124:127], v[44:47], v[140:143], v[124:127]\n\t"
        "ds_read_b128 v[140:143], v166 offset:21504\n\t"
        "ds_read_b128 v[80:83], v167 offset:4096\n\t"
        "ds_read_b128 v[84:87], v167 offset:5120\n\t"
        "s_waitcnt lgkmcnt(10)\n\t"
        "v_mfma_f32_16x16x32_bf16 v[112:115], v[52:55], v[144:147], v[112:115]\n\t"
        "ds_read_b128 v[144:147], v166 offset:22528\n\t"
        "s_waitcnt lgkmcnt(10)\n\t"
        "v_mfma_f32_16x16x32_bf16 v[116:119], v[52:55], v[148:151], v[116:119]\n\t"
        "ds_read_b128 v[148:151], v166 offset:23552\n\t"
        "s_waitcnt lgkmcnt(9)\n\t"
        "v_mfma_f32_16x16x32_bf16 v[120:123], v[52:55], v[152:155], v[120:123]\n\t"
        "ds_read_b128 v[152:155], v166 offset:24576\n\t"
        "s_waitcnt lgkmcnt(8)\n\t"
        "v_mfma_f32_16x16x32_bf16 v[124:127], v[52:55], v[156:159], v[124:127]\n\t"
        "ds_read_b128 v[156:159], v166 offset:25600\n\t"
        "s_waitcnt lgkmcnt(5)\n\t"
        "s_add_u32 s88, s90, 0\n\t"
        "buffer_store_dwordx4 v[80:83], v163, s[36:39], s88 offen offset:128 nt\n\t"
        "v_mfma_f32_16x16x32_bf16 v[112:115], v[60:63], v[136:139], v[112:115]\n\t"
        "ds_read_b128 v[136:139], v166 offset:26624\n\t"
        "v_mfma_f32_16x16x32_bf16 v[116:119], v[60:63], v[140:143], v[116:119]\n\t"
        "ds_read_b128 v[140:143], v166 offset:27648\n\t"
        "s_waitcnt lgkmcnt(5)\n\t"
        "v_mfma_f32_16x16x32_bf16 v[120:123], v[60:63], v[144:147], v[120:123]\n\t"
        "ds_read_b128 v[144:147], v166 offset:0\n\t"
        "s_add_u32 s88, s90, 73728\n\t"
        "buffer_store_dwordx4 v[84:87], v163, s[36:39], s88 offen offset:128 nt\n\t"
        "s_waitcnt lgkmcnt(5)\n\t"
        "v_mfma_f32_16x16x32_bf16 v[124:127], v[60:63], v[148:151], v[124:127]\n\t"
        "ds_read_b128 v[148:151], v166 offset:1024\n\t"
        "s_waitcnt lgkmcnt(5)\n\t"
        "v_mfma_f32_16x16x32_bf16 v[112:115], v[4:7], v[152:155], v[112:115]\n\t"
        "ds_read_b128 v[152:155], v166 offset:2048\n\t"
        "s_waitcnt lgkmcnt(5)\n\t"
        "v_mfma_f32_16x16x32_bf16 v[116:119], v[4:7], v[156:159], v[116:119]\n\t"
        "ds_read_b128 v[156:159], v166 offset:3072\n\t"
        "s_waitcnt lgkmcnt(5)\n\t"
        "v_mfma_f32_16x16x32_bf16 v[120:123], v[4:7], v[136:139], v[120:123]\n\t"
        "s_waitcnt lgkmcnt(4)\n\t"
        "v_mfma_f32_16x16x32_bf16 v[124:127], v[4:7], v[140:143], v[124:127]\n\t"
        "s_waitcnt lgkmcnt(3)\n\t"
        "v_mfma_f32_16x16x32_bf16 v[112:115], v[12:15], v[144:147], v[112:115]\n\t"
        "s_waitcnt lgkmcnt(2)\n\t"
        "v_mfma_f32_16x16x32_bf16 v[116:119], v[12:15], v[148:151], v[116:119]\n\t"
        "s_waitcnt lgkmcnt(1)\n\t"
        "v_mfma_f32_16x16x32_bf16 v[120:123], v[12:15], v[152:155], v[120:123]\n\t"
        "s_waitcnt lgkmcnt(0)\n\t"
        "v_mfma_f32_16x16x32_bf16 v[124:127], v[12:15], v[156:159], v[124:127]\n\t"
        "s_nop 3\n\t"
        "v_cvt_pk_bf16_f32 v132, v112, v116\n\t"
        "s_nop 2\n\t"
        "v_cvt_pk_bf16_f32 v133, v120, v124\n\t"
        "ds_write_b64 v162, v[132:133] offset:6144\n\t"
        "v_cvt_pk_bf16_f32 v134, v113, v117\n\t"
        "v_cvt_pk_bf16_f32 v135, v121, v125\n\t"
        "ds_write_b64 v162, v[134:135] offset:6272\n\t"
        "v_cvt_pk_bf16_f32 v132, v114, v118\n\t"
        "v_cvt_pk_bf16_f32 v133, v122, v126\n\t"
        "ds_write_b64 v162, v[132:133] offset:6400\n\t"
        "v_cvt_pk_bf16_f32 v134, v115, v119\n\t"
        "v_cvt_pk_bf16_f32 v135, v123, v127\n\t"
        "ds_write_b64 v162, v[134:135] offset:6528\n\t"
        "ds_read_b128 v[72:75], v167 offset:6144\n\t"
        "ds_read_b128 v[76:79], v167 offset:7168\n\t"
        "s_waitcnt lgkmcnt(1)\n\t"
        "s_add_u32 s88, s91, 0\n\t"
        "buffer_store_dwordx4 v[72:75], v163, s[36:39], s88 offen offset:128 nt\n\t"
        "s_waitcnt lgkmcnt(0)\n\t"
        "s_add_u32 s88, s91, 73728\n\t"
        "buffer_store_dwordx4 v[76:79], v163, s[36:39], s88 offen offset:128 nt\n\t"
        "6:\n\t"
        : "+{v167}"(tid)
        : [karg] "s"(__builtin_amdgcn_kernarg_segment_ptr()), [bid] "s"(blockIdx.x), "s"((unsigned)(uintptr_t)smem)
        : "memory", "vcc", "scc", "m0", "v0", "v1", "v2", "v3", "v4", "v5", "v6", "v7", "v8", "v9", "v10", "v11", "v12", "v13", "v14", "v15", "v16", "v17", "v18", "v19", "v20", "v21", "v22", "v23", "v24", "v25", "v26", "v27", "v28", "v29", "v30", "v31", "v32", "v33", "v34", "v35", "v36", "v37", "v38", "v39", "v40", "v41", "v42", "v43", "v44", "v45", "v46", "v47", "v48", "v49", "v50", "v51", "v52", "v53", "v54", "v55", "v56", "v57", "v58", "v59", "v60", "v61", "v62", "v63", "v64", "v65", "v66", "v67", "v68", "v69", "v70", "v71", "v72", "v73", "v74", "v75", "v76", "v77", "v78", "v79", "v80", "v81", "v82", "v83", "v84", "v85", "v86", "v87", "v88", "v89", "v90", "v91", "v92", "v93", "v94", "v95", "v96", "v97", "v98", "v99", "v100", "v101", "v102", "v103", "v104", "v105", "v106", "v107", "v108", "v109", "v110", "v111", "v112", "v113", "v114", "v115", "v116", "v117", "v118", "v119", "v120", "v121", "v122", "v123", "v124", "v125", "v126", "v127", "v128", "v129", "v130", "v131", "v132", "v133", "v134", "v135", "v136", "v137", "v138", "v139", "v140", "v141", "v142", "v143", "v144", "v145", "v146", "v147", "v148", "v149", "v150", "v151", "v152", "v153", "v154", "v155", "v156", "v157", "v158", "v159", "v160", "v161", "v162", "v163", "v164", "v165", "v166", "s8", "s9", "s10", "s11", "s12", "s13", "s14", "s15", "s16", "s17", "s18", "s19", "s20", "s21", "s22", "s23", "s24", "s25", "s26", "s27", "s28", "s29", "s30", "s31", "s32", "s33", "s34", "s35", "s36", "s37", "s38", "s39", "s40", "s41", "s42", "s43", "s44", "s45", "s46", "s47", "s48", "s49", "s50", "s51", "s52", "s53", "s54", "s55", "s56", "s57", "s58", "s59", "s60", "s61", "s62", "s63", "s64", "s65", "s66", "s67", "s68", "s69", "s70", "s71", "s72", "s73", "s74", "s75", "s76", "s77", "s78", "s79", "s80", "s81", "s82", "s83", "s84", "s85", "s86", "s87", "s88", "s89", "s90", "s91", "s92", "s93", "s94", "s95", "s96", "s97", "s98", "s99", "s100", "s101");
}

extern "C" void kernel_launch(void* const* d_in, const int* in_sizes, int n_in,
                              void* d_out, int out_size, void* d_ws, size_t ws_size,
                              hipStream_t stream) {
    w4a16_gemm_kernel<<<256, 768, 0, stream>>>((const unsigned short*)d_in[0], (const int*)d_in[1],
                                               (const unsigned short*)d_in[2], (const unsigned short*)d_in[3],
                                               (unsigned short*)d_out);
}
```
